```python
import torch

def _softmax(x, axis=-1):
    return torch.softmax(x, dim=axis)

def _gelu(x):
    return torch.nn.functional.gelu(x, approximate='tanh')

def _silu(x):
    return torch.nn.functional.silu(x)

def _sigmoid(x):
    return torch.sigmoid(x)

def _layernorm(x, axis=-1, eps=1e-05):
    m = x.mean(dim=axis, keepdim=True)
    v = x.var(dim=axis, keepdim=True, unbiased=False)
    return (x - m) / torch.sqrt(v + eps)

def _scatter_add(tgt, idx, src, axis=0):
    return tgt.clone().index_add_(axis, idx, src)

def _segment_sum(src, seg, n):
    out = torch.zeros((n,) + src.shape[1:], dtype=src.dtype)
    return out.index_add_(0, seg, src)

def _pad(x, pw):
    flat = tuple((v for pair in reversed(pw) for v in pair))
    return torch.nn.functional.pad(x, flat)
kernel_name = 'concat_heads__ssm_conv_h3__memory_aug_recurrent_mem_tr__prompt'

def setup_inputs():

    _w = lambda a: (lambda t: t.to(torch.bfloat16) if t.is_floating_point() else t)(torch.as_tensor(a))

    def _A_setup():
        B, S, D = (1, 2048, 512)
        ks = 4
        return {'q': _w((lambda *s: torch.randn(*s, dtype=torch.bfloat16) * 0.05)(B, S, D)), 'k': _w((lambda *s: torch.randn(*s, dtype=torch.bfloat16) * 0.4)(B, S, D)), 'v': _w((lambda *s: torch.randn(*s, dtype=torch.bfloat16) * 0.4)(B, S, D)), 'shift_k': _w((lambda *s: torch.randn(*s, dtype=torch.bfloat16) * 0.4)(D, ks) * 0.5), 'ssm_k': _w((lambda *s: torch.randn(*s, dtype=torch.bfloat16) * 0.4)(D, S) * (1.0 / S ** 0.5)), 'ssm_D': _w((lambda *s: torch.randn(*s, dtype=torch.bfloat16) * 0.4)(D))}
    _A_in = _A_setup()

    def _B_setup():
        B, S, Dm = (1, 2048, 512)
        H, Dh = (32, 128)
        M, seg = (8, 32)
        seg = min(seg, S)
        S = S // seg * seg
        return {'x': _w((lambda *s: torch.randn(*s, dtype=torch.bfloat16) * 0.05)(B, S, Dm)), 'mem0': _w((lambda *s: torch.randn(*s, dtype=torch.bfloat16) * 0.4)(B, M, Dm)), 'Wq': _w((lambda *s: torch.randn(*s, dtype=torch.bfloat16) * 6.4)(Dm, H, Dh) * Dm ** (-0.5)), 'Wk': _w((lambda *s: torch.randn(*s, dtype=torch.bfloat16) * 6.4)(Dm, H, Dh) * Dm ** (-0.5)), 'Wv': _w((lambda *s: torch.randn(*s, dtype=torch.bfloat16) * 0.4)(Dm, H, Dh) * Dm ** (-0.5)), 'Wo': _w((lambda *s: torch.randn(*s, dtype=torch.bfloat16) * 0.4)(H, Dh, Dm) * (H * Dh) ** (-0.5)), 'seg_len': seg}
    _B_in = _B_setup()
    return {'A_q': _A_in['q'], 'A_k': _A_in['k'], 'A_v': _A_in['v'], 'A_shift_k': _A_in['shift_k'], 'A_ssm_k': _A_in['ssm_k'], 'A_ssm_D': _A_in['ssm_D'], 'B_x': _B_in['x'], 'B_mem0': _B_in['mem0'], 'B_Wq': _B_in['Wq'], 'B_Wk': _B_in['Wk'], 'B_Wv': _B_in['Wv'], 'B_Wo': _B_in['Wo'], 'B_seg_len': _B_in['seg_len']}

def reference(A_q, A_k, A_v, A_shift_k, A_ssm_k, A_ssm_D, B_x, B_mem0, B_Wq, B_Wk, B_Wv, B_Wo, B_seg_len):

    def _A_ref(q, k, v, shift_k, ssm_k, ssm_D):
        B, S, D = q.shape
        ks = shift_k.shape[-1]
        k_pad = _pad(k, ((0, 0), (ks - 1, 0), (0, 0)))
        k_shift = torch.zeros_like(k)
        for t in range(ks):
            k_shift = k_shift + k_pad[:, t:t + S, :] * shift_k[:, ks - 1 - t][None, None, :]
        kv = k_shift * v
        L = 2 * S
        kv_pad = _pad(kv, ((0, 0), (0, S), (0, 0)))
        kf_pad = _pad(ssm_k, ((0, 0), (0, S)))
        kv_f = (lambda x, axes=(-2, -1): torch.fft.rfft2(x.float(), dim=axes))(kv_pad[:, None, :, :], axes=(1, 2))
        kf_f = (lambda x, axes=(-2, -1): torch.fft.rfft2(x.float(), dim=axes))(kf_pad[:, None, :], axes=(1, 2))
        kf_f = (lambda x, *d: x.permute(*d))(kf_f, 1, 2, 0)
        y_f = kv_f * kf_f[None, :, :, :]
        y = (lambda x, s, axes=(-2, -1): torch.fft.irfft2(x, s=s, dim=axes).to(torch.bfloat16))(y_f, (1, L), axes=(1, 2))[:, 0, :S, :]
        y = y + kv * ssm_D[None, None, :]
        return q * y

    def _B_ref(x, mem0, Wq, Wk, Wv, Wo, seg_len):
        B, S, Dm = x.shape
        H, Dh = (Wq.shape[1], Wq.shape[2])
        M = mem0.shape[1]
        scale = Dh ** (-0.5)
        n_seg = S // seg_len
        L = M + seg_len
        idx = torch.arange(L)
        mask = (idx[:, None] < M) | (idx[:, None] >= idx[None, :])
        mem = mem0
        outs = []
        for s in range(n_seg):
            xs = x[:, s * seg_len:(s + 1) * seg_len, :].clone()
            h = torch.concatenate([mem, xs], axis=1)
            q = torch.einsum('btd,dhe->bhte', h, Wq)
            k = torch.einsum('btd,dhe->bhte', h, Wk)
            v = torch.einsum('btd,dhe->bhte', h, Wv)
            scores = torch.einsum('bhqe,bhke->bhqk', q, k) * scale
            scores = torch.where(mask[None, None], scores, -1000000000.0)
            p = _softmax(scores, axis=-1)
            o = torch.einsum('bhqk,bhke->bhqe', p, v)
            o = torch.einsum('bhte,hed->btd', o, Wo)
            mem = o[:, :M, :].clone()
            outs.append(o[:, M:, :])
        return torch.concatenate(outs, axis=1)
    a = _A_ref(A_q, A_k, A_v, A_shift_k, A_ssm_k, A_ssm_D)
    b = _B_ref(B_x, B_mem0, B_Wq, B_Wk, B_Wv, B_Wo, B_seg_len)
    return torch.concatenate([a, b], axis=-1)
```

```cpp
#include <hip/hip_runtime.h>
#include <hip/hip_bf16.h>
#include <stdint.h>

typedef unsigned v4u __attribute__((ext_vector_type(4)));
typedef unsigned v2u __attribute__((ext_vector_type(2)));
typedef float v4f __attribute__((ext_vector_type(4)));
typedef float v16f __attribute__((ext_vector_type(16)));
typedef __bf16 v8bf __attribute__((ext_vector_type(8)));
typedef __bf16 v16bf __attribute__((ext_vector_type(16)));
typedef __bf16 v2bf __attribute__((ext_vector_type(2)));
typedef short v4s __attribute__((ext_vector_type(4)));
typedef unsigned v8u __attribute__((ext_vector_type(8)));
typedef __attribute__((address_space(3))) unsigned char* ldsp;
typedef __amdgpu_buffer_rsrc_t rsrc_t;

#define DEV __device__ __forceinline__

constexpr unsigned WS_CTL = 0;
constexpr unsigned WS_GF = 4096;
constexpr unsigned WS_HF = WS_GF + 64 * 64 * 4;
constexpr unsigned WS_XF = WS_HF + 64 * 32 * 16;
constexpr unsigned WS_MEMT = 128 * 1024;
constexpr unsigned WS_PART = WS_MEMT + 64 * 8192;
constexpr unsigned WS_INIT_END = WS_PART + 512 * 1024;
constexpr unsigned WS_KM = 2u << 20;
constexpr unsigned WS_VM = WS_KM + (4u << 20);
constexpr unsigned WS_QX = 16u << 20;
constexpr unsigned WS_KX = WS_QX + (16u << 20);
constexpr unsigned WS_VX = WS_KX + (16u << 20);
constexpr unsigned WS_OX = WS_VX + (16u << 20);
constexpr unsigned WS_END = WS_OX + (16u << 20);

constexpr int NSEG = 64;
constexpr int SC1 = 16;
constexpr unsigned POISON = 0xFFFFFFFFu;
constexpr unsigned SPIN_CAP = 1u << 19;
constexpr int LDS_BYTES = 160 * 1024;

struct Args {
    const unsigned char* aq; const unsigned char* ak; const unsigned char* av; const unsigned char* ashift;
    const unsigned char* assm; const unsigned char* ad; const unsigned char* bx; const unsigned char* bmem;
    const unsigned char* wq; const unsigned char* wk; const unsigned char* wv; const unsigned char* wo;
    unsigned char* out; unsigned char* ws;
    unsigned mode; unsigned pad0;
};

DEV rsrc_t mkrsrc(const void* p, unsigned bytes) { return __builtin_amdgcn_make_buffer_rsrc((void*)p, 0, bytes, 0x00020000); }
DEV v4u ld128(rsrc_t r, unsigned off) { return __builtin_amdgcn_raw_buffer_load_b128(r, off, 0, 0); }
DEV v4u ld128c(rsrc_t r, unsigned off) { return __builtin_amdgcn_raw_buffer_load_b128(r, off, 0, SC1); }
DEV unsigned ld32c(rsrc_t r, unsigned off) { return __builtin_amdgcn_raw_buffer_load_b32(r, off, 0, SC1); }
DEV void st128c(rsrc_t r, unsigned off, v4u v) { __builtin_amdgcn_raw_buffer_store_b128(v, r, off, 0, SC1); }
DEV void st64c(rsrc_t r, unsigned off, v2u v) { __builtin_amdgcn_raw_buffer_store_b64(v, r, off, 0, SC1); }
DEV void st32c(rsrc_t r, unsigned off, unsigned v) { __builtin_amdgcn_raw_buffer_store_b32(v, r, off, 0, SC1); }

DEV v4u lr128(ldsp p) { return *(const __attribute__((address_space(3))) v4u*)p; }
DEV v2u lr64(ldsp p) { return *(const __attribute__((address_space(3))) v2u*)p; }
DEV void lw128(ldsp p, v4u v) { *(__attribute__((address_space(3))) v4u*)p = v; }
DEV void lw64(ldsp p, v2u v) { *(__attribute__((address_space(3))) v2u*)p = v; }
DEV v2u ltr(ldsp p) { return __builtin_bit_cast(v2u, __builtin_amdgcn_ds_read_tr16_b64_v4i16((__attribute__((address_space(3))) v4s*)p)); }

DEV void bar() { asm volatile("s_waitcnt lgkmcnt(0)\n\ts_barrier" ::: "memory"); }
DEV void wait_vm0() { asm volatile("s_waitcnt vmcnt(0)" ::: "memory"); }
DEV void wait_lgkm0() { asm volatile("s_waitcnt lgkmcnt(0)" ::: "memory"); }
DEV void cfence() { asm volatile("" ::: "memory"); }

DEV unsigned pk2(float a, float b) { v2bf t; t[0] = (__bf16)a; t[1] = (__bf16)b; return __builtin_bit_cast(unsigned, t); }
DEV v8bf as8(v4u v) { return __builtin_bit_cast(v8bf, v); }
DEV v8bf as8(v2u a, v2u b) { v4u t = {a.x, a.y, b.x, b.y}; return __builtin_bit_cast(v8bf, t); }
DEV v16bf as16(v4u a, v4u b) { v8u t = {a.x, a.y, a.z, a.w, b.x, b.y, b.z, b.w}; return __builtin_bit_cast(v16bf, t); }
DEV v4s as4s(v2u a) { return __builtin_bit_cast(v4s, a); }
DEV float bf_lo(unsigned u) { return __uint_as_float(u << 16); }
DEV float bf_hi(unsigned u) { return __uint_as_float(u & 0xFFFF0000u); }
DEV float bfr(float x) { return (float)(__bf16)x; }

DEV v4f mfma32(v8bf a, v8bf b, v4f c) { return __builtin_amdgcn_mfma_f32_16x16x32_bf16(a, b, c, 0, 0, 0); }
DEV v4f mfma16(v2u a, v2u b, v4f c) { return __builtin_amdgcn_mfma_f32_16x16x16bf16_1k(as4s(a), as4s(b), c, 0, 0, 0); }
DEV v4f smf(v8bf a, v16bf b, v4f c, int idx) { return __builtin_amdgcn_smfmac_f32_16x16x64_bf16(a, b, c, idx, 0, 0); }

DEV float swadd32(float a, float b) {
    auto s = __builtin_amdgcn_permlane32_swap(__float_as_uint(a), __float_as_uint(b), false, false);
    return __uint_as_float(s[0]) + __uint_as_float(s[1]);
}
DEV float xmax_g(float x) {
    auto s = __builtin_amdgcn_permlane16_swap(__float_as_uint(x), __float_as_uint(x), false, false);
    x = fmaxf(__uint_as_float(s[0]), __uint_as_float(s[1]));
    auto t = __builtin_amdgcn_permlane32_swap(__float_as_uint(x), __float_as_uint(x), false, false);
    return fmaxf(__uint_as_float(t[0]), __uint_as_float(t[1]));
}
DEV float xsum_g(float x) {
    auto s = __builtin_amdgcn_permlane16_swap(__float_as_uint(x), __float_as_uint(x), false, false);
    x = __uint_as_float(s[0]) + __uint_as_float(s[1]);
    auto t = __builtin_amdgcn_permlane32_swap(__float_as_uint(x), __float_as_uint(x), false, false);
    return __uint_as_float(t[0]) + __uint_as_float(t[1]);
}

DEV bool spin_fail(rsrc_t ws, unsigned& spins) {
    ++spins;
    if ((spins & 255u) == 0) {
        if (spins >= SPIN_CAP) { st32c(ws, WS_CTL, 0u); return true; }
        if (__builtin_amdgcn_readfirstlane(ld32c(ws, WS_CTL)) != POISON) return true;
    }
    return false;
}
DEV bool wait_flag(rsrc_t ws, unsigned off) {
    unsigned spins = 0;
    for (;;) {
        cfence();
        const unsigned f = ld32c(ws, off);
        if (__all(f != POISON)) return true;
        if (spin_fail(ws, spins)) return false;
    }
}

constexpr float SM_C = 0.08838834764831845f * 1.4426950408889634f;
constexpr float MASK_T = -999292928.0f * 1.4426950408889634f;

template <bool VT>
DEV void g_role(const Args& A, ldsp lds, int gi, int wave, int lane) {
    const rsrc_t ws = mkrsrc(A.ws, WS_END);
    const rsrc_t xr = mkrsrc(A.bx, 2048 * 512 * 2);
    const int g = lane >> 4, c16 = lane & 15;
    const int h0 = VT ? 2 * (gi - 32) : gi, h1 = VT ? h0 + 1 : gi;
    v8bf W[2][2][16];
#pragma unroll
    for (int m = 0; m < 2; ++m) {
        const rsrc_t wr = mkrsrc(VT ? A.wv : (m == 0 ? A.wq : A.wk), 512 * 4096 * 2);
        const unsigned hb = (m == 0 ? h0 : h1) * 256;
        v4u st[32];
#pragma unroll
        for (int i = 0; i < 32; ++i) st[i] = ld128(wr, (i * 16 + wave * 4 + g) * 8192 + hb + c16 * 16);
        if (m == 1) bar();
#pragma unroll
        for (int i = 0; i < 32; ++i) {
            const int row = i * 16 + wave * 4 + g;
            const int f = (row & 3) | (((row >> 3) & 1) << 2);
            lw128(lds + row * 256 + (((c16 >> 1) ^ f) * 32) + (c16 & 1) * 16, st[i]);
        }
        bar();
        const int q = (lane >> 2) & 3, p = lane & 3;
        const int f = q | ((g & 1) << 2);
#pragma unroll
        for (int tt = 0; tt < 2; ++tt) {
            ldsp b = lds + (8 * g + q) * 256 + (((2 * wave + tt) ^ f) * 32) + p * 8;
#pragma unroll
            for (int kk = 0; kk < 16; ++kk) {
                W[m][tt][kk] = as8(ltr(b + kk * 8192), ltr(b + kk * 8192 + 1024));
                asm volatile("" : "+a"(W[m][tt][kk]));
            }
        }
    }
    bar();
    v4u xs[8];
    auto xload = [&](int s) {
#pragma unroll
        for (int i = 0; i < 8; ++i) xs[i] = ld128(xr, (s * 32 + wave * 8 + i) * 1024 + lane * 16);
    };
    auto xwrite = [&](int s) {
        ldsp b = lds + (s % 3) * 32768;
#pragma unroll
        for (int i = 0; i < 8; ++i) { const int row = wave * 8 + i; lw128(b + row * 1024 + ((lane ^ (row & 15)) << 4), xs[i]); }
    };
    xload(0); xwrite(0); xload(1);
    int rb[4];
#pragma unroll
    for (int j = 0; j < 4; ++j) rb[j] = c16 * 1024 + ((((j << 2) | g) ^ c16) << 4);
    const unsigned o0 = VT ? WS_VX + h0 * 8192 : WS_QX + h0 * 8192;
    const unsigned o1 = VT ? WS_VX + h1 * 8192 : WS_KX + h1 * 8192;
    for (int s = 0; s < NSEG; ++s) {
        wait_vm0();
        if (s + 1 < NSEG) xwrite(s + 1);
        if (s + 2 < NSEG) xload(s + 2);
        bar();
        if (s > 0 && threadIdx.x == 0) st32c(ws, WS_GF + ((s - 1) * 64 + gi) * 4, 1u);
        ldsp xb = lds + (s % 3) * 32768;
        v4f acc[2][2][2];
#pragma unroll
        for (int m = 0; m < 2; ++m)
#pragma unroll
            for (int tt = 0; tt < 2; ++tt)
#pragma unroll
                for (int t = 0; t < 2; ++t) acc[m][tt][t] = v4f{0, 0, 0, 0};
#pragma unroll
        for (int kk = 0; kk < 16; ++kk) {
#pragma unroll
            for (int t = 0; t < 2; ++t) {
                const v8bf xf = as8(lr128(xb + rb[kk & 3] + (kk >> 2) * 256 + t * 16384));
#pragma unroll
                for (int m = 0; m < 2; ++m)
#pragma unroll
                    for (int tt = 0; tt < 2; ++tt) {
                        asm volatile("" : "+a"(W[m][tt][kk]));
                        acc[m][tt][t] = VT ? mfma32(xf, W[m][tt][kk], acc[m][tt][t]) : mfma32(W[m][tt][kk], xf, acc[m][tt][t]);
                    }
            }
        }
#pragma unroll
        for (int m = 0; m < 2; ++m)
#pragma unroll
            for (int j = 0; j < 2; ++j) {
                const v4f a = VT ? acc[m][j][0] : acc[m][0][j], b = VT ? acc[m][j][1] : acc[m][1][j];
                const v4u o = {pk2(a[0], a[1]), pk2(a[2], a[3]), pk2(b[0], b[1]), pk2(b[2], b[3])};
                const int chunk = VT ? 2 * wave + j : j * 4 + wave;
                st128c(ws, (m == 0 ? o0 : o1) + s * (32 * 8192) + chunk * 1024 + lane * 16, o);
            }
    }
    wait_vm0();
    bar();
    if (threadIdx.x == 0) st32c(ws, WS_GF + ((NSEG - 1) * 64 + gi) * 4, 1u);
}

constexpr int L_WV = 0, L_MEM = 131072, L_X = L_MEM + 8192, L_KX = L_X + 4096, L_VX = L_KX + 8192, L_O = L_VX + 8192;

DEV void h_role(const Args& A, ldsp lds, int h, int wave, int lane) {
    const rsrc_t ws = mkrsrc(A.ws, WS_END);
    const int g = lane >> 4, c16 = lane & 15, q = (lane >> 2) & 3, p = lane & 3;
    v16bf Wqk[4][8];
    v16bf Wo[8][2];
    {
        ldsp my = lds + L_WV + wave * 32768;
        const int wrow = lane >> 2, wch = lane & 3;
        const int wsig = ((wrow >> 2) & 1) | (((wrow >> 3) & 1) << 2);
        const int wa = (wrow >> 2) * 256 + ((((wrow & 3) * 2 + (wch >> 1)) ^ wsig) * 32) + (wch & 1) * 16;
        int ra[2][2];
#pragma unroll
        for (int rd = 0; rd < 2; ++rd)
#pragma unroll
            for (int t = 0; t < 2; ++t) {
                const int row = 16 * (g >> 1) + 4 * (g & 1) + (q & 1) + 8 * (q >> 1) + 2 * rd;
                const int sig = ((row >> 2) & 1) | (((row >> 3) & 1) << 2);
                ra[rd][t] = (row >> 2) * 256 + ((((row & 3) * 2 + t) ^ sig) * 32) + p * 8;
            }
        v4u st[32];
        auto wload = [&](const unsigned char* w) {
            const rsrc_t wr = mkrsrc(w, 512 * 4096 * 2);
#pragma unroll
            for (int i = 0; i < 32; ++i) st[i] = ld128(wr, (i * 16 + wrow) * 8192 + h * 256 + wave * 64 + wch * 16);
        };
        auto wwrite = [&]() {
#pragma unroll
            for (int i = 0; i < 32; ++i) lw128(my + i * 1024 + wa, st[i]);
        };
        auto frag = [&](int t, int kb) {
            ldsp b = my + kb * 4096;
            const v2u r0 = ltr(b + ra[0][t]), r1 = ltr(b + ra[1][t]), r2 = ltr(b + 2048 + ra[0][t]), r3 = ltr(b + 2048 + ra[1][t]);
            return as16(v4u{r0.x, r0.y, r1.x, r1.y}, v4u{r2.x, r2.y, r3.x, r3.y});
        };
        wload(A.wq);
        wwrite(); cfence();
        wload(A.wk);
        wait_lgkm0();
#pragma unroll
        for (int t = 0; t < 2; ++t)
#pragma unroll
            for (int kb = 0; kb < 8; ++kb) { Wqk[t][kb] = frag(t, kb); asm volatile("" : "+a"(Wqk[t][kb])); }
        wait_lgkm0();
        wwrite(); cfence();
        wload(A.wv);
        wait_lgkm0();
#pragma unroll
        for (int t = 0; t < 2; ++t)
#pragma unroll
            for (int kb = 0; kb < 8; ++kb) { Wqk[2 + t][kb] = frag(t, kb); asm volatile("" : "+a"(Wqk[2 + t][kb])); }
        wait_lgkm0();
        wwrite(); cfence();
        const rsrc_t wor = mkrsrc(A.wo, 4096 * 512 * 2);
        v4u so[4][8];
        auto oload = [&](int j) {
#pragma unroll
            for (int i = 0; i < 8; ++i) so[j][i] = ld128(wor, (h * 128 + j * 32 + i * 4 + g) * 1024 + wave * 256 + c16 * 16);
        };
        oload(0); oload(1);
        wait_lgkm0();
        {
            v16bf vt[8][2];
#pragma unroll
            for (int kb = 0; kb < 8; ++kb)
#pragma unroll
                for (int t = 0; t < 2; ++t) vt[kb][t] = frag(t, kb);
            wait_lgkm0();
#pragma unroll
            for (int kb = 0; kb < 8; ++kb)
#pragma unroll
                for (int t = 0; t < 2; ++t) {
                    const v8u u = __builtin_bit_cast(v8u, vt[kb][t]);
                    lw128(my + (kb * 2 + t) * 2048 + lane * 16, v4u{u[0], u[1], u[2], u[3]});
                    lw128(my + (kb * 2 + t) * 2048 + 1024 + lane * 16, v4u{u[4], u[5], u[6], u[7]});
                }
        }
        cfence();
        oload(2); oload(3);
        ldsp sl = lds + L_MEM + wave * 8192;
        v4u wo4[8][4];
#pragma unroll
        for (int j = 0; j < 4; ++j) {
            wait_lgkm0();
#pragma unroll
            for (int i = 0; i < 8; ++i) {
                const int row = i * 4 + g;
                const int f = (row & 1) | (((row >> 2) & 1) << 1) | (((row >> 3) & 1) << 2);
                lw128(sl + row * 256 + (((c16 >> 1) ^ f) * 32) + (c16 & 1) * 16, so[j][i]);
            }
            wait_lgkm0();
#pragma unroll
            for (int ct = 0; ct < 8; ++ct) {
                v2u r[2];
#pragma unroll
                for (int rd = 0; rd < 2; ++rd) {
                    const int row = 16 * (g >> 1) + 4 * (g & 1) + (q & 1) + 8 * (q >> 1) + 2 * rd;
                    const int f = (row & 1) | (((row >> 2) & 1) << 1) | (((row >> 3) & 1) << 2);
                    r[rd] = ltr(sl + row * 256 + ((ct ^ f) * 32) + p * 8);
                }
                wo4[ct][j] = v4u{r[0].x, r[0].y, r[1].x, r[1].y};
            }
        }
#pragma unroll
        for (int ct = 0; ct < 8; ++ct)
#pragma unroll
            for (int kb = 0; kb < 2; ++kb) Wo[ct][kb] = as16(wo4[ct][2 * kb], wo4[ct][2 * kb + 1]);
        bar();
    }
    const int sidx = (lane & 8) ? 0xEEEE : 0x4444;
    const float ninf = -__builtin_inff();
    const int a_mw = (wave * 2048 + lane * 16) ^ ((g & 1) << 6);
    const int a_m0 = g * 256 + (p >> 1) * 128 + ((0 ^ (g & 1)) * 64) + q * 16 + (p & 1) * 8;
    const int a_m1 = g * 256 + (p >> 1) * 128 + ((1 ^ (g & 1)) * 64) + q * 16 + (p & 1) * 8;
    const int a_xw = (32 * wave + c16) * 32 + ((g ^ (c16 >> 2)) * 8);
    const int a_xr = (4 * g + q) * 32 + ((p ^ g) * 8);
    const int a_ow = (4 * wave + (g >> 1)) * 128 + (c16 & 7) * 16 + (g & 1) * 8;
    const unsigned pcol = 128 * wave + 16 * (lane >> 5) + c16;
    const unsigned a_part = WS_PART + (pcol >> 2) * 4096 + h * 128 + (pcol & 3) * 32 + (g & 1) * 16;

    auto kvfetch = [&](int s) -> bool {
        if (!wait_flag(ws, WS_GF + (s * 64 + ((lane & 1) ? 32 + (h >> 1) : h)) * 4)) return false;
        const unsigned b = (s * 32 + h) * 8192 + wave * 2048 + lane * 16;
        const v4u k0 = ld128c(ws, WS_KX + b), k1 = ld128c(ws, WS_KX + b + 1024);
        const v4u v0 = ld128c(ws, WS_VX + b), v1 = ld128c(ws, WS_VX + b + 1024);
        lw128(lds + L_KX + wave * 2048 + lane * 16, k0); lw128(lds + L_KX + wave * 2048 + 1024 + lane * 16, k1);
        lw128(lds + L_VX + wave * 2048 + lane * 16, v0); lw128(lds + L_VX + wave * 2048 + 1024 + lane * 16, v1);
        return true;
    };
    if (!kvfetch(0)) return;

    for (int s = 0; s < NSEG; ++s) {
        {
            v4u m0, m1;
            unsigned spins = 0;
            const unsigned mo = WS_MEMT + s * 8192 + wave * 2048 + lane * 16;
            for (;;) {
                cfence();
                m0 = ld128c(ws, mo); m1 = ld128c(ws, mo + 1024);
                const unsigned mx = max(max(max(m0.x, m0.y), max(m0.z, m0.w)), max(max(m1.x, m1.y), max(m1.z, m1.w)));
                if (__all(mx != POISON)) break;
                if (spin_fail(ws, spins)) return;
            }
            lw128(lds + L_MEM + a_mw, m0); lw128(lds + L_MEM + a_mw + 1024, m1);
        }
        bar();
        v4f aq[4], av[2];
#pragma unroll
        for (int i = 0; i < 4; ++i) { aq[i] = v4f{0, 0, 0, 0}; asm volatile("" : "+v"(aq[i])); }
#pragma unroll
        for (int i = 0; i < 2; ++i) { av[i] = v4f{0, 0, 0, 0}; asm volatile("" : "+v"(av[i])); }
#pragma unroll
        for (int kb = 0; kb < 8; ++kb) {
            const v8bf a = as8(ltr(lds + L_MEM + kb * 1024 + a_m0), ltr(lds + L_MEM + kb * 1024 + a_m1));
            ldsp wv = lds + L_WV + wave * 32768 + kb * 4096 + lane * 16;
            const v16bf b0 = as16(lr128(wv), lr128(wv + 1024)), b1 = as16(lr128(wv + 2048), lr128(wv + 3072));
#pragma unroll
            for (int i = 0; i < 4; ++i) { asm volatile("" : "+a"(Wqk[i][kb])); aq[i] = smf(a, Wqk[i][kb], aq[i], sidx); }
            av[0] = smf(a, b0, av[0], sidx);
            av[1] = smf(a, b1, av[1], sidx);
        }
#pragma unroll
        for (int t = 0; t < 2; ++t) {
            float x[4];
#pragma unroll
            for (int r = 0; r < 4; ++r) x[r] = swadd32(aq[t][r], aq[2 + t][r]);
            lw64(lds + L_X + t * 512 + a_xw, v2u{pk2(x[0], x[1]), pk2(x[2], x[3])});
        }
        v2u vmA, vmB;
        {
            float u[4];
#pragma unroll
            for (int r = 0; r < 4; ++r) u[r] = swadd32(av[0][r], av[1][r]);
            const unsigned u0 = pk2(u[0], u[1]), u1 = pk2(u[2], u[3]);
            auto s0 = __builtin_amdgcn_permlane32_swap(0u, u0, false, false);
            auto s1 = __builtin_amdgcn_permlane32_swap(0u, u1, false, false);
            vmA = v2u{s0[0], s1[0]}; vmB = v2u{s0[1], s1[1]};
        }
        bar();
        if (s + 1 < NSEG) {
            v8bf xf[4];
#pragma unroll
            for (int ks = 0; ks < 4; ++ks) xf[ks] = as8(ltr(lds + L_X + ks * 1024 + a_xr), ltr(lds + L_X + ks * 1024 + 512 + a_xr));
            const float ci = g < 2 ? ninf : 0.f;
            v4f sm = {ci, ci, ci, ci}, s0 = {0, 0, 0, 0}, s1 = {0, 0, 0, 0};
#pragma unroll
            for (int ks = 0; ks < 4; ++ks) {
                sm = mfma32(xf[ks], xf[ks], sm);
                s0 = mfma32(as8(lr128(lds + L_KX + ks * 1024 + lane * 16)), xf[ks], s0);
                s1 = mfma32(as8(lr128(lds + L_KX + 4096 + ks * 1024 + lane * 16)), xf[ks], s1);
            }
            const v8bf vx0 = as8(lr128(lds + L_VX + wave * 2048 + lane * 16)), vx1 = as8(lr128(lds + L_VX + wave * 2048 + 1024 + lane * 16));
            float mx = fmaxf(fmaxf(fmaxf(sm[0], sm[1]), fmaxf(sm[2], sm[3])), fmaxf(fmaxf(fmaxf(s0[0], s0[1]), fmaxf(s0[2], s0[3])), fmaxf(fmaxf(s1[0], s1[1]), fmaxf(s1[2], s1[3]))));
            mx = xmax_g(mx) * SM_C;
            float sum = 0.f;
#pragma unroll
            for (int r = 0; r < 4; ++r) {
                sm[r] = __builtin_amdgcn_exp2f(__builtin_fmaf(sm[r], SM_C, -mx));
                s0[r] = __builtin_amdgcn_exp2f(__builtin_fmaf(s0[r], SM_C, -mx));
                s1[r] = __builtin_amdgcn_exp2f(__builtin_fmaf(s1[r], SM_C, -mx));
                sum += (sm[r] + s0[r]) + s1[r];
            }
            const v8bf px = as8(v4u{pk2(s0[0], s0[1]), pk2(s0[2], s0[3]), pk2(s1[0], s1[1]), pk2(s1[2], s1[3])});
            const v2u pm = {pk2(sm[0], sm[1]), pk2(sm[2], sm[3])};
            v4f o0 = mfma32(vx0, px, v4f{0, 0, 0, 0}), o1 = mfma32(vx1, px, v4f{0, 0, 0, 0});
            o0 = mfma16(vmA, pm, o0); o1 = mfma16(vmB, pm, o1);
            const float rs = __builtin_amdgcn_rcpf(xsum_g(sum));
            if ((lane & 8) == 0) {
                lw64(lds + L_O + a_ow, v2u{pk2(o0[0] * rs, o0[1] * rs), pk2(o0[2] * rs, o0[3] * rs)});
                lw64(lds + L_O + 256 + a_ow, v2u{pk2(o1[0] * rs, o1[1] * rs), pk2(o1[2] * rs, o1[3] * rs)});
            }
            bar();
            const v8bf oa0 = as8(lr128(lds + L_O + lane * 16)), oa1 = as8(lr128(lds + L_O + 1024 + lane * 16));
            const unsigned tag = s & 1;
#pragma unroll
            for (int j = 0; j < 4; ++j) {
                v4f c0 = {0, 0, 0, 0}, c1 = {0, 0, 0, 0};
                asm volatile("" : "+v"(c0)); asm volatile("" : "+v"(c1));
                c0 = smf(oa0, Wo[2 * j][0], c0, sidx); c1 = smf(oa0, Wo[2 * j + 1][0], c1, sidx);
                c0 = smf(oa1, Wo[2 * j][1], c0, sidx); c1 = smf(oa1, Wo[2 * j + 1][1], c1, sidx);
                v4u o;
#pragma unroll
                for (int r = 0; r < 4; ++r) o[r] = (__float_as_uint(swadd32(c0[r], c1[r])) & ~1u) | tag;
                st128c(ws, a_part + j * (8 * 4096), o);
            }
        }
        {
            const v2u r0 = ltr(lds + L_X + wave * 1024 + a_xr), r1 = ltr(lds + L_X + wave * 1024 + 512 + a_xr);
            const unsigned b = (s * 32 + h) * 2048 + wave * 512;
            if (lane & 8) st128c(ws, WS_KM + b + g * 128 + (lane & 7) * 16, v4u{r0.x, r0.y, r1.x, r1.y});
            if (lane >= 32) st128c(ws, WS_VM + b + (g & 1) * 256 + c16 * 16, v4u{vmA.x, vmA.y, vmB.x, vmB.y});
        }
        if (s + 1 < NSEG) { if (!kvfetch(s + 1)) return; }
        wait_vm0();
        if (lane == 0) st32c(ws, WS_HF + ((s * 32 + h) * 4 + wave) * 4, 1u);
    }
}

template <int TAG>
DEV bool r_step(rsrc_t ws, int s, int jw, int lane) {
    v4u c[4];
    unsigned spins = 0;
    const unsigned base = WS_PART + jw * 4096 + lane * 16;
    for (;;) {
        cfence();
#pragma unroll
        for (int u = 0; u < 4; ++u) c[u] = ld128c(ws, base + u * 1024);
        unsigned m;
        if (TAG) {
            m = c[0].x & c[0].y & c[0].z & c[0].w;
#pragma unroll
            for (int u = 1; u < 4; ++u) m = m & c[u].x & c[u].y & c[u].z & c[u].w;
            m = ~m;
        } else {
            m = c[0].x | c[0].y | c[0].z | c[0].w;
#pragma unroll
            for (int u = 1; u < 4; ++u) m = m | c[u].x | c[u].y | c[u].z | c[u].w;
        }
        if (__all((m & 1u) == 0)) break;
        if (spin_fail(ws, spins)) return false;
    }
    float x[4];
#pragma unroll
    for (int r = 0; r < 4; ++r) x[r] = (__uint_as_float(c[0][r]) + __uint_as_float(c[1][r])) + (__uint_as_float(c[2][r]) + __uint_as_float(c[3][r]));
    float y0 = swadd32(x[0], x[2]), y1 = swadd32(x[1], x[3]);
    {
        auto a = __builtin_amdgcn_permlane16_swap(__float_as_uint(y0), __float_as_uint(y0), false, false);
        y0 = __uint_as_float(a[0]) + __uint_as_float(a[1]);
        auto b = __builtin_amdgcn_permlane16_swap(__float_as_uint(y1), __float_as_uint(y1), false, false);
        y1 = __uint_as_float(b[0]) + __uint_as_float(b[1]);
    }
    y0 += __uint_as_float(__builtin_amdgcn_update_dpp(0u, __float_as_uint(y0), 0x128, 0xF, 0xF, false));
    y1 += __uint_as_float(__builtin_amdgcn_update_dpp(0u, __float_as_uint(y1), 0x128, 0xF, 0xF, false));
    const unsigned d = min(pk2(y0, y1), 0xFFFFFFFEu);
    auto e = __builtin_amdgcn_permlane32_swap(d, d, false, false);
    if (lane < 8) st64c(ws, WS_MEMT + (s + 1) * 8192 + jw * 64 + lane * 8, v2u{e[0], e[1]});
    return true;
}
DEV void r_role(const Args& A, int jw, int lane) {
    const rsrc_t ws = mkrsrc(A.ws, WS_END);
    if (lane < 8) {
        const unsigned short* m = (const unsigned short*)A.bmem;
        const int col = jw * 4 + (lane >> 1), r0 = (lane & 1) * 4;
        const unsigned d0 = min((unsigned)m[(r0 + 0) * 512 + col] | ((unsigned)m[(r0 + 1) * 512 + col] << 16), 0xFFFFFFFEu);
        const unsigned d1 = min((unsigned)m[(r0 + 2) * 512 + col] | ((unsigned)m[(r0 + 3) * 512 + col] << 16), 0xFFFFFFFEu);
        st64c(ws, WS_MEMT + jw * 64 + lane * 8, v2u{d0, d1});
    }
    for (int s = 0; s < NSEG - 1; s += 2) {
        if (!r_step<0>(ws, s, jw, lane)) return;
        if (s + 1 < NSEG - 1) { if (!r_step<1>(ws, s + 1, jw, lane)) return; }
    }
}

DEV void x_role(const Args& A, int h, int wave, int lane) {
    const rsrc_t ws = mkrsrc(A.ws, WS_END);
    const int g = lane >> 4, c16 = lane & 15, qt = wave & 1;
    const float ninf = -__builtin_inff();
    for (int s = wave >> 1; s < NSEG; s += 2) {
        if (!wait_flag(ws, WS_GF + (s * 64 + ((lane & 1) ? 32 + (h >> 1) : h)) * 4)) return;
        const unsigned b = (s * 32 + h) * 8192 + lane * 16;
        v4u qf[4], kf[8], vf[8];
#pragma unroll
        for (int i = 0; i < 4; ++i) qf[i] = ld128c(ws, WS_QX + b + (qt * 4 + i) * 1024);
#pragma unroll
        for (int i = 0; i < 8; ++i) kf[i] = ld128c(ws, WS_KX + b + i * 1024);
#pragma unroll
        for (int i = 0; i < 8; ++i) vf[i] = ld128c(ws, WS_VX + b + i * 1024);
        if (!wait_flag(ws, WS_HF + ((s * 32 + h) * 4 + (lane & 3)) * 4)) return;
        const unsigned bm = (s * 32 + h) * 2048;
        v4u km[4], vm[4];
#pragma unroll
        for (int i = 0; i < 4; ++i) km[i] = ld128c(ws, WS_KM + bm + i * 512 + g * 128 + (lane & 7) * 16);
#pragma unroll
        for (int i = 0; i < 4; ++i) vm[i] = ld128c(ws, WS_VM + bm + i * 512 + (g & 1) * 256 + c16 * 16);
        v4f s0 = {0, 0, 0, 0}, s1 = {0, 0, 0, 0}, sm = {0, 0, 0, 0};
#pragma unroll
        for (int ks = 0; ks < 4; ++ks) {
            s0 = mfma32(as8(kf[ks]), as8(qf[ks]), s0);
            s1 = mfma32(as8(kf[4 + ks]), as8(qf[ks]), s1);
        }
#pragma unroll
        for (int ks = 0; ks < 4; ++ks) sm = mfma32(as8(km[ks]), as8(qf[ks]), sm);
        const int qx = 16 * qt + c16;
        float mx = ninf;
#pragma unroll
        for (int r = 0; r < 4; ++r) {
            sm[r] = g < 2 ? sm[r] * SM_C : ninf;
            s0[r] = (4 * g + r <= qx) ? s0[r] * SM_C : MASK_T;
            s1[r] = (16 + 4 * g + r <= qx) ? s1[r] * SM_C : MASK_T;
            mx = fmaxf(mx, fmaxf(sm[r], fmaxf(s0[r], s1[r])));
        }
        mx = xmax_g(mx);
        float sum = 0.f;
#pragma unroll
        for (int r = 0; r < 4; ++r) {
            sm[r] = __builtin_amdgcn_exp2f(sm[r] - mx);
            s0[r] = __builtin_amdgcn_exp2f(s0[r] - mx);
            s1[r] = __builtin_amdgcn_exp2f(s1[r] - mx);
            sum += (sm[r] + s0[r]) + s1[r];
        }
        const v8bf px = as8(v4u{pk2(s0[0], s0[1]), pk2(s0[2], s0[3]), pk2(s1[0], s1[1]), pk2(s1[2], s1[3])});
        const v2u pm = {pk2(sm[0], sm[1]), pk2(sm[2], sm[3])};
        const float rs = __builtin_amdgcn_rcpf(xsum_g(sum));
#pragma unroll
        for (int u = 0; u < 4; ++u) {
            v4f o0 = mfma32(as8(vf[2 * u]), px, v4f{0, 0, 0, 0}), o1 = mfma32(as8(vf[2 * u + 1]), px, v4f{0, 0, 0, 0});
            o0 = mfma16(v2u{vm[u].x, vm[u].y}, pm, o0);
            o1 = mfma16(v2u{vm[u].z, vm[u].w}, pm, o1);
            const v4u o = {pk2(o0[0] * rs, o0[1] * rs), pk2(o0[2] * rs, o0[3] * rs), pk2(o1[0] * rs, o1[1] * rs), pk2(o1[2] * rs, o1[3] * rs)};
            st128c(ws, WS_OX + (s * 32 + h) * 8192 + (qt * 4 + u) * 1024 + lane * 16, o);
        }
        wait_vm0();
        if (lane == 0) st32c(ws, WS_XF + ((s * 2 + qt) * 32 + h) * 4, 1u);
    }
}

DEV void o_role(const Args& A, ldsp lds, int set, int cg, int wave, int lane) {
    const rsrc_t ws = mkrsrc(A.ws, WS_END);
    const rsrc_t wor = mkrsrc(A.wo, 4096 * 512 * 2);
    const int g = lane >> 4, c16 = lane & 15, q = (lane >> 2) & 3, p = lane & 3;
    const int qt = set & 1;
    v8bf W[8][4][2];
    {
        ldsp my = lds + wave * 8192;
        const int wrow = lane >> 2, wch = lane & 3;
#pragma unroll
        for (int hh = 0; hh < 8; ++hh) {
            v4u st[8];
#pragma unroll
            for (int i = 0; i < 8; ++i) st[i] = ld128(wor, ((wave * 8 + hh) * 128 + i * 16 + wrow) * 1024 + cg * 64 + wch * 16);
            wait_lgkm0();
#pragma unroll
            for (int i = 0; i < 8; ++i) {
                const int row = i * 16 + wrow;
                lw128(my + row * 64 + ((((wch >> 1) ^ ((row >> 2) & 1))) * 32) + (wch & 1) * 16, st[i]);
            }
            wait_lgkm0();
#pragma unroll
            for (int u = 0; u < 4; ++u)
#pragma unroll
                for (int ct = 0; ct < 2; ++ct) {
                    ldsp b = my + (32 * u + 4 * g + q) * 64 + ((ct ^ (g & 1)) * 32) + p * 8;
                    W[hh][u][ct] = as8(ltr(b), ltr(b + 1024));
                    asm volatile("" : "+a"(W[hh][u][ct]));
                }
        }
    }
    bar();
    ldsp red = lds + 32768;
    for (int s = set >> 1; s < NSEG; s += 2) {
        if (!wait_flag(ws, WS_XF + ((s * 2 + qt) * 32 + wave * 8 + (lane & 7)) * 4)) return;
        v4u of[8][4];
#pragma unroll
        for (int hh = 0; hh < 8; ++hh)
#pragma unroll
            for (int u = 0; u < 4; ++u) of[hh][u] = ld128c(ws, WS_OX + (s * 32 + wave * 8 + hh) * 8192 + (qt * 4 + u) * 1024 + lane * 16);
        v4f c0 = {0, 0, 0, 0}, c1 = {0, 0, 0, 0};
#pragma unroll
        for (int hh = 0; hh < 8; ++hh)
#pragma unroll
            for (int u = 0; u < 4; ++u) {
                asm volatile("" : "+a"(W[hh][u][0])); asm volatile("" : "+a"(W[hh][u][1]));
                c0 = mfma32(W[hh][u][0], as8(of[hh][u]), c0);
                c1 = mfma32(W[hh][u][1], as8(of[hh][u]), c1);
            }
        ldsp rb = red + ((s >> 1) & 1) * 16384;
        *(__attribute__((address_space(3))) v4f*)(rb + wave * 2304 + c16 * 144 + g * 16) = c0;
        *(__attribute__((address_space(3))) v4f*)(rb + wave * 2304 + c16 * 144 + 64 + g * 16) = c1;
        bar();
        {
            const int tok = threadIdx.x >> 4, cp = threadIdx.x & 15;
            float a0 = 0.f, a1 = 0.f;
#pragma unroll
            for (int w = 0; w < 4; ++w) {
                const v2u t = lr64(rb + w * 2304 + tok * 144 + cp * 8);
                a0 += __uint_as_float(t.x); a1 += __uint_as_float(t.y);
            }
            *(unsigned*)(A.out + (size_t)(s * 32 + qt * 16 + tok) * 2048 + 1024 + cg * 64 + cp * 4) = pk2(a0, a1);
        }
    }
}

constexpr int A_PS = 1568, A_UC = 4 * A_PS;
constexpr int A_CS = 4160, A_WW = 4 * A_CS;
constexpr int A_YS = 1056, A_YC = 4 * A_YS;
constexpr int A_U = 0, A_W = A_U + 8 * A_UC, A_Y = A_W + 4 * A_WW;
DEV float bfsel(const v4u& v, int c) { return (c & 1) ? bf_hi(v[c >> 1]) : bf_lo(v[c >> 1]); }
DEV void a_role(const Args& A, ldsp lds, int ai, int wave, int lane) {
    const int tid = threadIdx.x;
    const rsrc_t kr = mkrsrc(A.ak, 2048 * 1024), vr = mkrsrc(A.av, 2048 * 1024), qr = mkrsrc(A.aq, 2048 * 1024);
    const rsrc_t sr = mkrsrc(A.assm, 512 * 4096);
    for (int i = tid; i < 8 * 4 * 32; i += 256) lw128(lds + A_U + (i >> 7) * A_UC + ((i >> 5) & 3) * A_PS + (i & 31) * 16, v4u{0, 0, 0, 0});
    if (lane < 16) lw128(lds + A_W + wave * A_WW + (lane >> 2) * A_CS + (lane & 3) * 16, v4u{0, 0, 0, 0});
    const int G = lane >> 4, q = (lane >> 2) & 3, p = lane & 3, kg = lane >> 5, tb = lane & 31;
    for (int grp = 0; grp < 4; ++grp) {
        const int ch0 = ai * 32 + grp * 8;
        {
            v4u kk[11], vv[8];
#pragma unroll
            for (int i = 0; i < 11; ++i) {
                const int row = 8 * tid - 3 + i;
                kk[i] = ld128(kr, (unsigned)(row < 0 ? 0 : row) * 1024 + ch0 * 2);
                if (i < 3 && row < 0) kk[i] = v4u{0, 0, 0, 0};
            }
#pragma unroll
            for (int i = 0; i < 8; ++i) vv[i] = ld128(vr, (8 * tid + i) * 1024 + ch0 * 2);
            const unsigned short* sk = (const unsigned short*)A.ashift + ch0 * 4;
#pragma unroll
            for (int c = 0; c < 8; ++c) {
                float w[4];
#pragma unroll
                for (int j = 0; j < 4; ++j) w[j] = __uint_as_float((unsigned)sk[c * 4 + j] << 16);
                float kv[8];
#pragma unroll
                for (int t = 0; t < 8; ++t) {
                    float a = bfr(0.f + bfr(bfsel(kk[t], c) * w[3]));
                    a = bfr(a + bfr(bfsel(kk[t + 1], c) * w[2]));
                    a = bfr(a + bfr(bfsel(kk[t + 2], c) * w[1]));
                    a = bfr(a + bfr(bfsel(kk[t + 3], c) * w[0]));
                    kv[t] = a * bfsel(vv[t], c);
                }
                lw128(lds + A_U + c * A_UC + (tid & 3) * A_PS + (32 + (tid >> 2)) * 16,
                      v4u{pk2(kv[0], kv[1]), pk2(kv[2], kv[3]), pk2(kv[4], kv[5]), pk2(kv[6], kv[7])});
            }
        }
        v4u qq[8];
#pragma unroll
        for (int i = 0; i < 8; ++i) qq[i] = ld128(qr, (8 * tid + i) * 1024 + ch0 * 2);
        bar();
        for (int cc = 0; cc < 2; ++cc) {
            const int c = wave * 2 + cc;
            ldsp wb = lds + A_W + wave * A_WW;
            wait_lgkm0();
#pragma unroll
            for (int i = 0; i < 4; ++i) {
                const int m0 = 8 * (lane + 64 * i);
                const unsigned ro = (ch0 + c) * 4096 + m0 * 2;
                const v4u a = ld128(sr, ro);
                v2u pr = __builtin_amdgcn_raw_buffer_load_b64(sr, m0 == 0 ? ro : ro - 8, 0, 0);
                if (m0 == 0) pr = v2u{0, 0};
                const unsigned e0 = __builtin_amdgcn_alignbit(pr.y, pr.x, 16), e1 = __builtin_amdgcn_alignbit(a.x, pr.y, 16), e2 = __builtin_amdgcn_alignbit(a.y, a.x, 16),
                               e3 = __builtin_amdgcn_alignbit(a.z, a.y, 16), e4 = __builtin_amdgcn_alignbit(a.w, a.z, 16);
                ldsp d = wb + (32 + m0) * 2;
                lw128(d, a);
                lw128(d + A_CS, v4u{e1, e2, e3, e4});
                lw128(d + 2 * A_CS, v4u{pr.y, a.x, a.y, a.z});
                lw128(d + 3 * A_CS, v4u{e0, e1, e2, e3});
            }
            wait_lgkm0();
            v16f y0, y1;
#pragma unroll
            for (int j = 0; j < 16; ++j) { y0[j] = 0.f; y1[j] = 0.f; }
            ldsp ab = wb + q * A_CS + (32 + 16 * (G & 1) - 8 * kg + 4 * p - 20) * 2;
            ldsp ub = lds + A_U + c * A_UC + kg * A_PS + (1 + tb) * 16;
#pragma unroll 4
            for (int d = 0; d < 32; ++d) {
#pragma unroll
                for (int hf = 0; hf < 2; ++hf) {
                    ldsp a0 = ab + d * 64 + (20 - 16 * hf) * 2;
                    const v8bf A0 = as8(ltr(a0), ltr(a0 - 8)), A1 = as8(ltr(a0 + 2048), ltr(a0 + 2048 - 8));
                    ldsp b0 = ub + hf * 2 * A_PS + (31 - d) * 16;
                    const v8bf B0 = as8(lr128(b0)), B1 = as8(lr128(b0 + 512));
                    y0 = __builtin_amdgcn_mfma_f32_32x32x16_bf16(A0, B0, y0, 0, 0, 0);
                    y1 = __builtin_amdgcn_mfma_f32_32x32x16_bf16(A0, B1, y1, 0, 0, 0);
                    y1 = __builtin_amdgcn_mfma_f32_32x32x16_bf16(A1, B0, y1, 0, 0, 0);
                }
            }
            ldsp yb = lds + A_Y + c * A_YC + tb * 16 + kg * 8;
#pragma unroll
            for (int b = 0; b < 4; ++b) {
                lw64(yb + b * A_YS, v2u{pk2(y0[4 * b], y0[4 * b + 1]), pk2(y0[4 * b + 2], y0[4 * b + 3])});
                lw64(yb + b * A_YS + 512, v2u{pk2(y1[4 * b], y1[4 * b + 1]), pk2(y1[4 * b + 2], y1[4 * b + 3])});
            }
        }
        bar();
        {
            const unsigned short* dp = (const unsigned short*)A.ad + ch0;
            v4u o[8];
#pragma unroll
            for (int t = 0; t < 8; ++t) o[t] = v4u{0, 0, 0, 0};
#pragma unroll
            for (int c = 0; c < 8; ++c) {
                const float D = __uint_as_float((unsigned)dp[c] << 16);
                const v4u yv = lr128(lds + A_Y + c * A_YC + (tid & 3) * A_YS + (tid >> 2) * 16);
                const v4u kv = lr128(lds + A_U + c * A_UC + (tid & 3) * A_PS + (32 + (tid >> 2)) * 16);
#pragma unroll
                for (int t = 0; t < 8; ++t) {
                    const float y2 = bfr(bfsel(yv, t) + bfr(bfsel(kv, t) * D));
                    const unsigned r = pk2(bfsel(qq[t], c) * y2, 0.f) & 0xFFFFu;
                    o[t][c >> 1] |= (c & 1) ? (r << 16) : r;
                }
            }
#pragma unroll
            for (int t = 0; t < 8; ++t) *(v4u*)(A.out + (size_t)(8 * tid + t) * 2048 + ch0 * 2) = o[t];
        }
        bar();
    }
}

__global__ void __launch_bounds__(256, 1) fused_kernel(Args A) {
    extern __shared__ __attribute__((aligned(16))) unsigned char lds_raw[];
    ldsp lds = (ldsp)lds_raw;
    const int b = blockIdx.x, lane = threadIdx.x & 63;
    const int wave = __builtin_amdgcn_readfirstlane(threadIdx.x >> 6);
    const int slot = b >> 3, xc = b & 3;
    if ((b & 7) < 4) {
        const int idx = slot * 4 + xc;
        if (idx < 32) { if (A.mode & 2) h_role(A, lds, idx, wave, lane); }
        else if (idx < 96) { if ((A.mode & 4) && wave < 2) r_role(A, (idx - 32) * 2 + wave, lane); }
        else { if (A.mode & 8) x_role(A, idx - 96, wave, lane); }
    } else {
        if (slot < 16) { if (A.mode & 16) o_role(A, lds, xc, slot, wave, lane); }
        else if (slot < 28) {
            const int gi = (slot - 16) * 4 + xc;
            if (A.mode & 1) { if (gi < 32) g_role<false>(A, lds, gi, wave, lane); else g_role<true>(A, lds, gi, wave, lane); }
        } else { if (A.mode & 32) a_role(A, lds, (slot - 28) * 4 + xc, wave, lane); }
    }
}

extern "C" void kernel_launch_mode(void* const* d_in, const int* in_sizes, int n_in, void* d_out, int out_size, void* d_ws, size_t ws_size,
                                   hipStream_t stream, unsigned mode, int do_memset) {
    Args a;
    a.aq = (const unsigned char*)d_in[0]; a.ak = (const unsigned char*)d_in[1]; a.av = (const unsigned char*)d_in[2];
    a.ashift = (const unsigned char*)d_in[3]; a.assm = (const unsigned char*)d_in[4]; a.ad = (const unsigned char*)d_in[5];
    a.bx = (const unsigned char*)d_in[6]; a.bmem = (const unsigned char*)d_in[7];
    a.wq = (const unsigned char*)d_in[8]; a.wk = (const unsigned char*)d_in[9]; a.wv = (const unsigned char*)d_in[10]; a.wo = (const unsigned char*)d_in[11];
    a.out = (unsigned char*)d_out; a.ws = (unsigned char*)d_ws;
    a.mode = mode; a.pad0 = 0;
    (void)hipFuncSetAttribute((const void*)fused_kernel, hipFuncAttributeMaxDynamicSharedMemorySize, LDS_BYTES);
    if (do_memset) (void)hipMemsetAsync(d_ws, 0xFF, WS_INIT_END, stream);
    hipLaunchKernelGGL(fused_kernel, dim3(256), dim3(256), LDS_BYTES, stream, a);
}

extern "C" void kernel_launch(void* const* d_in, const int* in_sizes, int n_in, void* d_out, int out_size, void* d_ws, size_t ws_size,
                              hipStream_t stream) {
    kernel_launch_mode(d_in, in_sizes, n_in, d_out, out_size, d_ws, ws_size, stream, 63u, 1);
}
```

```cpp
#include <hip/hip_runtime.h>
#include <hip/hip_bf16.h>
#include <stdint.h>

typedef unsigned v4u __attribute__((ext_vector_type(4)));
typedef unsigned v2u __attribute__((ext_vector_type(2)));
typedef float v4f __attribute__((ext_vector_type(4)));
typedef float v16f __attribute__((ext_vector_type(16)));
typedef __bf16 v8bf __attribute__((ext_vector_type(8)));
typedef __bf16 v16bf __attribute__((ext_vector_type(16)));
typedef __bf16 v2bf __attribute__((ext_vector_type(2)));
typedef short v4s __attribute__((ext_vector_type(4)));
typedef unsigned v8u __attribute__((ext_vector_type(8)));
typedef unsigned v16u __attribute__((ext_vector_type(16)));
typedef unsigned v32u __attribute__((ext_vector_type(32)));
typedef __attribute__((address_space(3))) unsigned char* ldsp;
typedef __amdgpu_buffer_rsrc_t rsrc_t;

#define DEV __device__ __forceinline__

constexpr unsigned WS_CTL = 0;
constexpr unsigned WS_ACK = 1024;
constexpr unsigned WS_PAR = 2048;
constexpr unsigned WS_GF = 4096;
constexpr unsigned WS_HF = WS_GF + 64 * 64 * 4;
constexpr unsigned WS_XF = WS_HF + 65 * 32 * 16;
constexpr unsigned WS_MEMT = 80 * 1024;
constexpr unsigned WS_INIT_END = WS_MEMT + 65536;
constexpr unsigned WS_PART = WS_MEMT + 4 * 65536;
constexpr unsigned WS_HB = 1408 * 1024;
constexpr unsigned WS_PROBE = 1536 * 1024;
constexpr unsigned WS_KM = 2u << 20;
constexpr unsigned WS_VM = WS_KM + (4u << 20);
constexpr unsigned WS_QX = 16u << 20;
constexpr unsigned WS_KX = WS_QX + (16u << 20);
constexpr unsigned WS_VX = WS_KX + (16u << 20);
constexpr unsigned WS_OX = WS_VX + (16u << 20);
constexpr unsigned WS_END = WS_OX + (16u << 20);

constexpr int NSEG = 64;
constexpr int SC1 = 16;
constexpr unsigned POISON = 0xFFFFFFFFu;
constexpr unsigned SPIN_CAP = 1u << 19;
constexpr int LDS_BYTES = 160 * 1024;

struct Args {
    const unsigned char* aq; const unsigned char* ak; const unsigned char* av; const unsigned char* ashift;
    const unsigned char* assm; const unsigned char* ad; const unsigned char* bx; const unsigned char* bmem;
    const unsigned char* wq; const unsigned char* wk; const unsigned char* wv; const unsigned char* wo;
    unsigned char* out; unsigned char* ws;
};

DEV rsrc_t mkrsrc(const void* p, unsigned bytes) { return __builtin_amdgcn_make_buffer_rsrc((void*)p, 0, bytes, 0x00020000); }
DEV v4u ld128(rsrc_t r, unsigned off) { return __builtin_amdgcn_raw_buffer_load_b128(r, off, 0, 0); }
DEV v4u ld128c(rsrc_t r, unsigned off) { return __builtin_amdgcn_raw_buffer_load_b128(r, off, 0, SC1); }
DEV unsigned ld32c(rsrc_t r, unsigned off) { return __builtin_amdgcn_raw_buffer_load_b32(r, off, 0, SC1); }
DEV void st128c(rsrc_t r, unsigned off, v4u v) { __builtin_amdgcn_raw_buffer_store_b128(v, r, off, 0, SC1); }
DEV void st64c(rsrc_t r, unsigned off, v2u v) { __builtin_amdgcn_raw_buffer_store_b64(v, r, off, 0, SC1); }
DEV void st32c(rsrc_t r, unsigned off, unsigned v) { __builtin_amdgcn_raw_buffer_store_b32(v, r, off, 0, SC1); }

DEV v4u lr128(ldsp p) { return *(const __attribute__((address_space(3))) v4u*)p; }
DEV v2u lr64(ldsp p) { return *(const __attribute__((address_space(3))) v2u*)p; }
DEV void lw128(ldsp p, v4u v) { *(__attribute__((address_space(3))) v4u*)p = v; }
DEV void lw64(ldsp p, v2u v) { *(__attribute__((address_space(3))) v2u*)p = v; }
DEV v2u ltr(ldsp p) { return __builtin_bit_cast(v2u, __builtin_amdgcn_ds_read_tr16_b64_v4i16((__attribute__((address_space(3))) v4s*)p)); }

DEV void dma16(rsrc_t r, unsigned voff, unsigned soff, unsigned ldsaddr) {
    asm volatile("s_mov_b32 m0, %0\n\ts_nop 0\n\tbuffer_load_dwordx4 %1, %2, %3 offen lds" :: "s"(ldsaddr), "v"(voff), "s"(r), "s"(soff) : "memory");
}
DEV void dma16c(rsrc_t r, unsigned voff, unsigned soff, unsigned ldsaddr) {
    asm volatile("s_mov_b32 m0, %0\n\ts_nop 0\n\tbuffer_load_dwordx4 %1, %2, %3 offen sc1 lds" :: "s"(ldsaddr), "v"(voff), "s"(r), "s"(soff) : "memory");
}
DEV void dma16k(rsrc_t r, unsigned voff, int soff, unsigned ldsbase, int ldsoff) {
    unsigned t;
    asm volatile("s_add_u32 m0, %1, %4\n\ts_mov_b32 %0, %5\n\tbuffer_load_dwordx4 %2, %3, %0 offen lds" : "=&s"(t) : "s"(ldsbase), "v"(voff), "s"(r), "i"(ldsoff), "i"(soff) : "memory", "scc");
}
DEV void dma16kc(rsrc_t r, unsigned voff, int soff, unsigned ldsbase, int ldsoff) {
    unsigned t;
    asm volatile("s_add_u32 m0, %1, %4\n\ts_mov_b32 %0, %5\n\tbuffer_load_dwordx4 %2, %3, %0 offen sc1 lds" : "=&s"(t) : "s"(ldsbase), "v"(voff), "s"(r), "i"(ldsoff), "i"(soff) : "memory", "scc");
}
template <int N> DEV void wait_vm_c() { asm volatile("s_waitcnt vmcnt(%0)" :: "n"(N) : "memory"); }
DEV void wait_vm(int n) { if (n >= 32) wait_vm_c<32>(); else if (n >= 24) wait_vm_c<24>(); else if (n >= 16) wait_vm_c<16>(); else if (n >= 8) wait_vm_c<8>(); else wait_vm_c<0>(); }
DEV void bar() { asm volatile("s_waitcnt lgkmcnt(0)\n\ts_barrier" ::: "memory"); }
DEV void wait_vm0() { asm volatile("s_waitcnt vmcnt(0)" ::: "memory"); }
DEV void wait_lgkm0() { asm volatile("s_waitcnt lgkmcnt(0)" ::: "memory"); }
DEV void cfence() { asm volatile("" ::: "memory"); }

DEV unsigned umax4(v4u v) { return max(max(v.x, v.y), max(v.z, v.w)); }
DEV unsigned pk2(float a, float b) { v2bf t; t[0] = (__bf16)a; t[1] = (__bf16)b; return __builtin_bit_cast(unsigned, t); }
DEV v8bf as8(v4u v) { return __builtin_bit_cast(v8bf, v); }
DEV v8bf as8(v2u a, v2u b) { v4u t = {a.x, a.y, b.x, b.y}; return __builtin_bit_cast(v8bf, t); }
DEV v16bf as16(v4u a, v4u b) { v8u t = {a.x, a.y, a.z, a.w, b.x, b.y, b.z, b.w}; return __builtin_bit_cast(v16bf, t); }
DEV v4s as4s(v2u a) { return __builtin_bit_cast(v4s, a); }
DEV float bf_lo(unsigned u) { return __uint_as_float(u << 16); }
DEV float bf_hi(unsigned u) { return __uint_as_float(u & 0xFFFF0000u); }
DEV float bfr(float x) { return (float)(__bf16)x; }

DEV v4f mfma32(v8bf a, v8bf b, v4f c) { return __builtin_amdgcn_mfma_f32_16x16x32_bf16(a, b, c, 0, 0, 0); }
DEV v4f mfma16(v2u a, v2u b, v4f c) { return __builtin_amdgcn_mfma_f32_16x16x16bf16_1k(as4s(a), as4s(b), c, 0, 0, 0); }
DEV v4f smf(v8bf a, v16bf b, v4f c, int idx) { return __builtin_amdgcn_smfmac_f32_16x16x64_bf16(a, b, c, idx, 0, 0); }

DEV float swadd32(float a, float b) {
    auto s = __builtin_amdgcn_permlane32_swap(__float_as_uint(a), __float_as_uint(b), false, false);
    return __uint_as_float(s[0]) + __uint_as_float(s[1]);
}
DEV float xmax_g(float x) {
    auto s = __builtin_amdgcn_permlane16_swap(__float_as_uint(x), __float_as_uint(x), false, false);
    x = fmaxf(__uint_as_float(s[0]), __uint_as_float(s[1]));
    auto t = __builtin_amdgcn_permlane32_swap(__float_as_uint(x), __float_as_uint(x), false, false);
    return fmaxf(__uint_as_float(t[0]), __uint_as_float(t[1]));
}
DEV float xsum_g(float x) {
    auto s = __builtin_amdgcn_permlane16_swap(__float_as_uint(x), __float_as_uint(x), false, false);
    x = __uint_as_float(s[0]) + __uint_as_float(s[1]);
    auto t = __builtin_amdgcn_permlane32_swap(__float_as_uint(x), __float_as_uint(x), false, false);
    return __uint_as_float(t[0]) + __uint_as_float(t[1]);
}

DEV bool spin_fail(rsrc_t ws, unsigned& spins) {
    ++spins;
    if ((spins & 255u) == 0) {
        if (spins >= SPIN_CAP) { st32c(ws, WS_CTL, 0u); return true; }
        if (__builtin_amdgcn_readfirstlane(ld32c(ws, WS_CTL)) != POISON) return true;
    }
    return false;
}
#define XO_SL 32
#define DP_FIRST 61
#define X_TIGHT 0
#define XO_LAST 2
DEV bool wait_flag(rsrc_t ws, unsigned off, bool relaxed) {
    unsigned spins = 0;
    for (;;) {
        cfence();
        const unsigned f = ld32c(ws, off);
        if (__all(f != POISON)) return true;
        if (spin_fail(ws, spins)) return false;
        if (relaxed) __builtin_amdgcn_s_sleep(XO_SL);
    }
}

DEV unsigned hb_issue(rsrc_t ws) {
    unsigned t;
    asm volatile("buffer_load_dword %0, off, %1, %2 sc1" : "=v"(t) : "s"(ws), "s"(WS_HB) : "memory");
    return t;
}
DEV unsigned hb_get(unsigned t) { unsigned r; asm volatile("v_readfirstlane_b32 %0, %1" : "=s"(r) : "v"(t) : "memory"); return r; }
DEV void hb_finish(rsrc_t ws, unsigned hb0, unsigned hb1, int lane) {
    for (unsigned spins = 0; hb1 == hb0 && spins < SPIN_CAP; ++spins) {
        __builtin_amdgcn_s_sleep(4);
        cfence();
        hb1 = __builtin_amdgcn_readfirstlane(ld32c(ws, WS_HB));
    }
    if (lane == 0) st32c(ws, WS_ACK + blockIdx.x * 4, 0u);
}
DEV void hb_wait(rsrc_t ws, int lane) {
    const unsigned hb0 = __builtin_amdgcn_readfirstlane(ld32c(ws, WS_HB));
    hb_finish(ws, hb0, hb0, lane);
}
DEV void beat_role(rsrc_t ws, unsigned beat, int lane) {
    if (lane == 0) st32c(ws, WS_ACK + blockIdx.x * 4, 0u);
    for (unsigned n = 0; n < SPIN_CAP; ++n) {
        ++beat;
        cfence();
        if (lane == 0) st32c(ws, WS_HB, beat);
        const v4u a = ld128c(ws, WS_ACK + lane * 16);
        if (__all(a.x != POISON && a.y != POISON && a.z != POISON && a.w != POISON)) break;
    }
}

template <int M> DEV void pf2_part(const Args& A, int j, int w8, int wave, int lane) {
    const int h = (j >> 3) * 4 + (j & 3);
    w8 += ((j >> 2) & 1) * 4;
    const unsigned vc = (w8 * 64 + (lane >> 4)) * 8192 + h * 256 + (lane & 15) * 16;
    const unsigned vr = (h * 128 + w8 * 16) * 1024 + lane * 16;
    auto cols = [&](const unsigned char* p) {
        const rsrc_t r = mkrsrc(p, 512 * 4096 * 2);
#pragma unroll
        for (int i = 0; i < 16; ++i) dma16k(r, vc, i * 32768, wave * 8192, 0);
    };
    if (M & 1) cols(A.wq);
    if (M & 2) cols(A.wk);
    if (M & 8) cols(A.wv);
    if (M & 4) {
        const rsrc_t ro = mkrsrc(A.wo, 4096 * 512 * 2);
#pragma unroll
        for (int i = 0; i < 16; ++i) dma16k(ro, vr, i * 1024, wave * 8192, 0);
    }
}

DEV unsigned probe_role(const Args& A, int pi, int lane) {
    const rsrc_t ws = mkrsrc(A.ws, WS_END);
    const unsigned base = WS_PROBE + pi * 32768;
    st32c(ws, base + (lane & 3) * 8192 + ((lane >> 2) & 3) * 512, 0u);
    unsigned best[2] = {0x7FFFu, 0x7FFFu};
#pragma unroll 1
    for (int r = 0; r < 8; ++r) {
#pragma unroll
        for (int par = 0; par < 2; ++par) {
            const unsigned off = base + par * 8192 + (r & 1) * 16384 + (r >> 1) * 512;
            asm volatile("s_waitcnt vmcnt(0) lgkmcnt(0)" ::: "memory");
            const unsigned long long t0 = __builtin_amdgcn_s_memtime();
            asm volatile("s_waitcnt lgkmcnt(0)" ::: "memory");
            unsigned v = ld32c(ws, off);
            asm volatile("s_waitcnt vmcnt(0)" : "+v"(v) :: "memory");
            const unsigned long long t1 = __builtin_amdgcn_s_memtime();
            asm volatile("s_waitcnt lgkmcnt(0)" ::: "memory");
            best[par] = min(best[par], (unsigned)(t1 - t0));
        }
    }
    return best[0] | (best[1] << 16);
}
DEV unsigned par_of(unsigned f) {
    unsigned m0 = 0xFFFFu, m1 = 0xFFFFu;
#pragma unroll
    for (int i = 0; i < 8; ++i) { const unsigned x = __builtin_amdgcn_readlane(f, i); m0 = min(m0, x & 0xFFFFu); m1 = min(m1, x >> 16); }
    return m1 < m0 ? 1u : 0u;
}
DEV bool get_par(rsrc_t ws, int lane, unsigned& par) {
    unsigned spins = 0, f;
    for (;;) {
        cfence();
        f = ld32c(ws, WS_PAR + (lane & 7) * 4);
        if (__all(f != POISON)) break;
        if (spin_fail(ws, spins)) return false;
    }
    par = par_of(f);
    return true;
}

constexpr float SM_C = 0.08838834764831845f * 1.4426950408889634f;
constexpr float MASK_T = -999292928.0f * 1.4426950408889634f;

DEV int vflag(int h) { return 32 + (h & 3) + 4 * (h >> 3); }
template <bool VT>
DEV void g_role(const Args& A, ldsp lds, int gi, int wave, int lane) {
    const rsrc_t ws = mkrsrc(A.ws, WS_END);
    const rsrc_t xr = mkrsrc(A.bx, 2048 * 512 * 2);
    const int g = lane >> 4, c16 = lane & 15;
    const int h0 = VT ? ((gi - 32) & 3) + 8 * ((gi - 32) >> 2) : gi, h1 = VT ? h0 + 4 : gi;
    v8bf W[2][2][16];
    v4u xs[8];
    auto xload = [&](int s) {
#pragma unroll
        for (int i = 0; i < 8; ++i) xs[i] = ld128(xr, (s * 32 + wave * 8 + i) * 1024 + lane * 16);
    };
    auto xwrite = [&](int s) {
        ldsp b = lds + (s % 3) * 32768;
#pragma unroll
        for (int i = 0; i < 8; ++i) { const int row = wave * 8 + i; lw128(b + row * 1024 + ((lane ^ (row & 15)) << 4), xs[i]); }
    };
    {
        unsigned hbv = 0, hb0 = 0;
        if (wave == 0) hbv = hb_issue(ws);
#pragma unroll
        for (int i = 0; i < 8; ++i) { const int row = wave * 8 + i; dma16k(xr, row * 1024 + ((lane ^ (row & 15)) << 4), 0, wave * 8192, i * 1024); }
        const rsrc_t w0 = mkrsrc(VT ? A.wv : A.wq, 512 * 4096 * 2), w1 = mkrsrc(VT ? A.wv : A.wk, 512 * 4096 * 2);
        const unsigned vb = (wave * 4 + g) * 8192 + ((((lane >> 1) & 7) ^ (g | ((wave >> 1) << 2))) * 32) + (lane & 1) * 16;
        const unsigned v0 = vb + h0 * 256, v1 = vb + h1 * 256;
        auto issue = [&](int c) {
#pragma unroll
            for (int i = 0; i < 8; ++i) dma16k(c < 4 ? w0 : w1, c < 4 ? v0 : v1, ((c & 3) * 128 + i * 16) * 8192, wave * 1024, 32768 + (c & 3) * 32768 + i * 4096);
        };
        const int q = (lane >> 2) & 3, p = lane & 3;
        const int f = q | ((g & 1) << 2);
#pragma unroll
        for (int c = 0; c < 4; ++c) issue(c);
#pragma unroll
        for (int c = 0; c < 8; ++c) {
            if (c == 0) wait_vm_c<24>(); else if (c < 6) wait_vm_c<16>(); else if (c == 6) wait_vm_c<16>(); else wait_vm_c<8>();
            if (c == 0 && wave == 0) hb0 = hb_get(hbv);
            bar();
            if (c >= 1 && c + 3 < 8) issue(c + 3);
            if (c == 5) { xload(1); if (wave == 0) hbv = hb_issue(ws); }
#pragma unroll
            for (int tt = 0; tt < 2; ++tt) {
                ldsp b = lds + 32768 + (c & 3) * 32768 + (8 * g + q) * 256 + (((2 * wave + tt) ^ f) * 32) + p * 8;
#pragma unroll
                for (int k4 = 0; k4 < 4; ++k4) {
                    W[c >> 2][tt][(c & 3) * 4 + k4] = as8(ltr(b + k4 * 8192), ltr(b + k4 * 8192 + 1024));
                    asm volatile("" : "+a"(W[c >> 2][tt][(c & 3) * 4 + k4]));
                }
            }
        }
        bar();

        if (wave == 0) { wait_vm0(); hb_finish(ws, hb0, hb_get(hbv), lane); }

    }
    int rb[4];
#pragma unroll
    for (int j = 0; j < 4; ++j) rb[j] = c16 * 1024 + ((((j << 2) | g) ^ c16) << 4);
    const unsigned o0 = VT ? WS_VX + h0 * 8192 : WS_QX + h0 * 8192;
    const unsigned o1 = VT ? WS_VX + h1 * 8192 : WS_KX + h1 * 8192;
    for (int s = 0; s < NSEG; ++s) {

        wait_vm0();

        if (s + 1 < NSEG) xwrite(s + 1);
        if (s + 2 < NSEG) xload(s + 2);
        bar();
        if (s > 0 && threadIdx.x == 0) st32c(ws, WS_GF + ((s - 1) * 64 + gi) * 4, 1u);

        ldsp xb = lds + (s % 3) * 32768;
        v4f acc[2][2][2];
#pragma unroll
        for (int m = 0; m < 2; ++m)
#pragma unroll
            for (int tt = 0; tt < 2; ++tt)
#pragma unroll
                for (int t = 0; t < 2; ++t) acc[m][tt][t] = v4f{0, 0, 0, 0};
        v4u xq[32];
        auto xrd = [&](int i) { xq[i] = lr128(xb + rb[(i >> 1) & 3] + (i >> 3) * 256 + (i & 1) * 16384); };
        xrd(0); xrd(1); xrd(2);
#pragma unroll
        for (int i = 0; i < 32; ++i) {
            const int kk = i >> 1, t = i & 1;
            if (i + 3 < 32) xrd(i + 3);
            __builtin_amdgcn_sched_barrier(0);
#pragma unroll
            for (int m = 0; m < 2; ++m)
#pragma unroll
                for (int tt = 0; tt < 2; ++tt) {
                    asm volatile("" : "+a"(W[m][tt][kk]));
                    acc[m][tt][t] = VT ? mfma32(as8(xq[i]), W[m][tt][kk], acc[m][tt][t]) : mfma32(W[m][tt][kk], as8(xq[i]), acc[m][tt][t]);
                }
            __builtin_amdgcn_sched_barrier(0);
        }
#pragma unroll
        for (int m = 0; m < 2; ++m)
#pragma unroll
            for (int j = 0; j < 2; ++j) {
                const v4f a = VT ? acc[m][j][0] : acc[m][0][j], b = VT ? acc[m][j][1] : acc[m][1][j];
                const v4u o = {pk2(a[0], a[1]), pk2(a[2], a[3]), pk2(b[0], b[1]), pk2(b[2], b[3])};
                const int chunk = VT ? 2 * wave + j : j * 4 + wave;
                st128c(ws, (m == 0 ? o0 : o1) + s * (32 * 8192) + chunk * 1024 + lane * 16, o);
            }

    }
    wait_vm0();
    bar();
    if (threadIdx.x == 0) st32c(ws, WS_GF + ((NSEG - 1) * 64 + gi) * 4, 1u);
}

constexpr int L_WV = 0, L_MEM = 131072, L_X = L_MEM + 8192, L_KX = L_X + 4096, L_VX = L_KX + 8192, L_O = L_VX + 8192;

DEV void h_run(const Args& A, ldsp lds, int h, int wave, int lane, const v16bf (&Wqk)[4][8], const v16bf (&Wo)[8][2]) {
    const rsrc_t ws = mkrsrc(A.ws, WS_END);
    const int g = lane >> 4, c16 = lane & 15, q = (lane >> 2) & 3, p = lane & 3;
    unsigned par;
    {
        unsigned spins = 0, f;
        const unsigned off = lane < 8 ? WS_PAR + lane * 4 : WS_GF + ((lane & 1) ? vflag(h) : h) * 4;
        for (;;) {
            cfence();
            f = ld32c(ws, off);
            if (__all(f != POISON)) break;
            if (spin_fail(ws, spins)) return;
        }
        par = par_of(f);
    }

    constexpr unsigned OOR = 0x80000000u;
    const int a_xw = (32 * wave + c16) * 32 + ((g ^ (c16 >> 2)) * 8);
    const int a_xr = (4 * g + q) * 32 + ((p ^ g) * 8);
    const int tok = 2 * (c16 >> 2) + (c16 & 1);
    v16u P0; v8u P1;
    P0[0] = WS_MEMT + (h & 3) * 16384 + par * 8192 + wave * 2048 + g * 256 + (c16 & 1) * 128 + (c16 >> 1) * 16;
    P0[1] = L_MEM + wave * 2048 + lane * 16;
    P0[2] = L_MEM + ((2 * wave) ^ 2) * 1024 + lane * 16;
    P0[3] = L_MEM + ((2 * wave) ^ 4) * 1024 + lane * 16;
    P0[4] = L_MEM + ((2 * wave) ^ 6) * 1024 + lane * 16;
    P0[5] = L_WV + wave * 32768 + lane * 16;
    P0[6] = L_X + a_xw;
    P0[7] = L_X + a_xr;
    P0[8] = L_X + wave * 1024 + a_xr;
    P0[9] = L_MEM + lane * 16;
    P0[10] = 0;
    P0[11] = (lane & 2) ? L_O + 2048 + tok * 32 + (g ^ 2) * 8 : L_O + wave * 512 + tok * 32 + g * 8;
    P0[12] = 0;
    P0[13] = WS_PART + (16 * wave + (c16 >> 2)) * 16384 + par * 8192 + (g >> 1) * 4096 + h * 128 + (g & 1) * 64 + (c16 & 3) * 16;
    P0[14] = (lane & 2) ? WS_KM + h * 2048 + wave * 512 + g * 128 + tok * 16 : OOR;
    P0[15] = WS_VM + h * 2048 + wave * 512 + lane * 8;
    P1[0] = WS_KX + h * 8192 + wave * 2048 + lane * 16;
    P1[1] = WS_GF + ((lane & 1) ? vflag(h) : h) * 4;
    P1[2] = lane == 0 ? WS_HF + (h * 4 + wave) * 4 : OOR;
    P1[3] = (lane & 1) ? 0xEEEE : 0x4444;
    P1[4] = P1[5] = P1[6] = P1[7] = 0u;
    dma16kc(ws, P1[0], 0, wave * 2048, L_KX); dma16kc(ws, P1[0], 1024, wave * 2048, L_KX + 1024);
    dma16kc(ws, P1[0], WS_VX - WS_KX, wave * 2048, L_VX); dma16kc(ws, P1[0], WS_VX - WS_KX + 1024, wave * 2048, L_VX + 1024);
    auto cat4 = [](v16bf a, v16bf b, v16bf c, v16bf d) {
        const v8u x = __builtin_bit_cast(v8u, a), y = __builtin_bit_cast(v8u, b), z = __builtin_bit_cast(v8u, c), w = __builtin_bit_cast(v8u, d);
        const v16u lo = __builtin_shufflevector(x, y, 0, 1, 2, 3, 4, 5, 6, 7, 8, 9, 10, 11, 12, 13, 14, 15);
        const v16u hi = __builtin_shufflevector(z, w, 0, 1, 2, 3, 4, 5, 6, 7, 8, 9, 10, 11, 12, 13, 14, 15);
        return __builtin_shufflevector(lo, hi, 0, 1, 2, 3, 4, 5, 6, 7, 8, 9, 10, 11, 12, 13, 14, 15, 16, 17, 18, 19, 20, 21, 22, 23, 24, 25, 26, 27, 28, 29, 30, 31);
    };
    const v32u a0 = cat4(Wqk[0][0], Wqk[0][1], Wqk[0][2], Wqk[0][3]), a1 = cat4(Wqk[0][4], Wqk[0][5], Wqk[0][6], Wqk[0][7]);
    const v32u a2 = cat4(Wqk[1][0], Wqk[1][1], Wqk[1][2], Wqk[1][3]), a3 = cat4(Wqk[1][4], Wqk[1][5], Wqk[1][6], Wqk[1][7]);
    const v32u a4 = cat4(Wqk[2][0], Wqk[2][1], Wqk[2][2], Wqk[2][3]), a5 = cat4(Wqk[2][4], Wqk[2][5], Wqk[2][6], Wqk[2][7]);
    const v32u a6 = cat4(Wqk[3][0], Wqk[3][1], Wqk[3][2], Wqk[3][3]), a7 = cat4(Wqk[3][4], Wqk[3][5], Wqk[3][6], Wqk[3][7]);
    const v32u o0 = cat4(Wo[0][0], Wo[0][1], Wo[1][0], Wo[1][1]), o1 = cat4(Wo[2][0], Wo[2][1], Wo[3][0], Wo[3][1]);
    const v32u o2 = cat4(Wo[4][0], Wo[4][1], Wo[5][0], Wo[5][1]), o3 = cat4(Wo[6][0], Wo[6][1], Wo[7][0], Wo[7][1]);
    const unsigned mk = L_KX + wave * 2048, mv = L_VX + wave * 2048;
    unsigned tsb = 0;
    asm volatile(
        "s_mov_b32 s40, 0\n"
        "s_mov_b32 s41, 0\n"
        "s_mov_b32 s42, 262144\n"
        "s_mov_b32 s43, 17039360\n"
        "s_mov_b32 s44, 0\n"
        "s_mov_b32 s45, 65536\n"
        "s_mov_b32 s46, 131072\n"
        "s_mov_b32 s47, 196608\n"
        "s_mov_b32 s48, 0\n"
        "s_mov_b32 s49, 256\n"
        "s_mov_b32 s50, 0\n"
        "s_mov_b32 s52, 0x3e0293ee\n"
        "LOOP%=:\n"
        "v_mov_b64 v[56:57], 0\n"
        "v_mov_b64 v[58:59], 0\n"
        "v_mov_b64 v[60:61], 0\n"
        "v_mov_b64 v[62:63], 0\n"
        "v_mov_b64 v[64:65], 0\n"
        "v_mov_b64 v[66:67], 0\n"
        "v_mov_b64 v[68:69], 0\n"
        "v_mov_b64 v[70:71], 0\n"
        "v_mov_b64 v[72:73], 0\n"
        "v_mov_b64 v[74:75], 0\n"
        "v_mov_b64 v[76:77], 0\n"
        "v_mov_b64 v[78:79], 0\n"
        "ds_read_b128 v[80:83], v5 offset:0\n"
        "ds_read_b128 v[84:87], v5 offset:1024\n"
        "ds_read_b128 v[88:91], v5 offset:2048\n"
        "ds_read_b128 v[92:95], v5 offset:3072\n"
        "ds_read_b128 v[96:99], v5 offset:4096\n"
        "ds_read_b128 v[100:103], v5 offset:5120\n"
        "ds_read_b128 v[104:107], v5 offset:6144\n"
        "ds_read_b128 v[108:111], v5 offset:7168\n"
        "ds_read_b128 v[112:115], v5 offset:8192\n"
        "ds_read_b128 v[116:119], v5 offset:9216\n"
        "ds_read_b128 v[120:123], v5 offset:10240\n"
        "ds_read_b128 v[124:127], v5 offset:11264\n"
        "s_mov_b32 s51, 0\n"
        "POLL%=:\n"
        "buffer_load_dwordx4 v[24:27], v0, s[36:39], s41 offen offset:0 sc1\n"
        "buffer_load_dwordx4 v[28:31], v0, s[36:39], s41 offen offset:1024 sc1\n"
        "s_waitcnt vmcnt(1)\n"
        "ds_write_b128 v1, v[24:27]\n"
        "v_max3_u32 v32, v24, v25, v26\n"
        "s_waitcnt vmcnt(0)\n"
        "ds_write_b128 v1, v[28:31] offset:1024\n"
        "v_max3_u32 v33, v28, v29, v30\n"
        "v_max3_u32 v32, v32, v27, v31\n"
        "v_max_u32 v32, v32, v33\n"
        "v_cmp_eq_u32 vcc, -1, v32\n"
        "s_cbranch_vccz GO%=\n"
        "s_add_u32 s51, s51, 1\n"
        "s_cmp_lt_u32 s51, 524288\n"
        "s_cbranch_scc1 POLL%=\n"
        "s_branch ABORT%=\n"
        "GO%=:\n"
        "v_smfmac_f32_16x16x64_bf16 v[56:59], v[24:27], a[0:7], v19\n"
        "v_smfmac_f32_16x16x64_bf16 v[64:67], v[24:27], a[128:135], v19\n"
        "v_smfmac_f32_16x16x64_bf16 v[60:63], v[24:27], a[64:71], v19\n"
        "v_smfmac_f32_16x16x64_bf16 v[68:71], v[24:27], a[192:199], v19\n"
        "s_waitcnt lgkmcnt(0)\n"
        "s_barrier\n"
        "ds_read_b128 v[32:35], v2 offset:0\n"
        "ds_read_b128 v[36:39], v2 offset:1024\n"
        "ds_read_b128 v[40:43], v3 offset:0\n"
        "ds_read_b128 v[44:47], v3 offset:1024\n"
        "ds_read_b128 v[48:51], v4 offset:0\n"
        "ds_read_b128 v[52:55], v4 offset:1024\n"
        "v_smfmac_f32_16x16x64_bf16 v[56:59], v[28:31], a[8:15], v19\n"
        "buffer_load_dword v10, v17, s[36:39], s49 offen sc1\n"
        "v_smfmac_f32_16x16x64_bf16 v[64:67], v[28:31], a[136:143], v19\n"
        "v_mov_b32 v12, 1\n"
        "v_smfmac_f32_16x16x64_bf16 v[60:63], v[28:31], a[72:79], v19\n"
        "buffer_store_dword v12, v18, s[36:39], s50 offen sc1\n"
        "v_smfmac_f32_16x16x64_bf16 v[68:71], v[28:31], a[200:207], v19\n"
        "v_smfmac_f32_16x16x64_bf16 v[72:75], v[24:27], v[80:87], v19\n"
        "v_smfmac_f32_16x16x64_bf16 v[76:79], v[24:27], v[88:95], v19\n"
        "ds_read_b128 v[80:83], v5 offset:12288\n"
        "v_smfmac_f32_16x16x64_bf16 v[72:75], v[28:31], v[96:103], v19\n"
        "ds_read_b128 v[84:87], v5 offset:13312\n"
        "v_smfmac_f32_16x16x64_bf16 v[76:79], v[28:31], v[104:111], v19\n"
        "ds_read_b128 v[88:91], v5 offset:14336\n"
        "s_waitcnt lgkmcnt(8)\n"
        "v_smfmac_f32_16x16x64_bf16 v[56:59], v[32:35], a[16:23], v19\n"
        "v_smfmac_f32_16x16x64_bf16 v[64:67], v[32:35], a[144:151], v19\n"
        "ds_read_b128 v[92:95], v5 offset:15360\n"
        "v_smfmac_f32_16x16x64_bf16 v[60:63], v[32:35], a[80:87], v19\n"
        "v_smfmac_f32_16x16x64_bf16 v[68:71], v[32:35], a[208:215], v19\n"
        "ds_read_b128 v[96:99], v5 offset:16384\n"
        "s_waitcnt lgkmcnt(9)\n"
        "v_smfmac_f32_16x16x64_bf16 v[56:59], v[36:39], a[24:31], v19\n"
        "v_smfmac_f32_16x16x64_bf16 v[64:67], v[36:39], a[152:159], v19\n"
        "ds_read_b128 v[100:103], v5 offset:17408\n"
        "v_smfmac_f32_16x16x64_bf16 v[60:63], v[36:39], a[88:95], v19\n"
        "v_smfmac_f32_16x16x64_bf16 v[68:71], v[36:39], a[216:223], v19\n"
        "ds_read_b128 v[104:107], v5 offset:18432\n"
        "s_waitcnt lgkmcnt(10)\n"
        "v_smfmac_f32_16x16x64_bf16 v[56:59], v[40:43], a[32:39], v19\n"
        "v_smfmac_f32_16x16x64_bf16 v[64:67], v[40:43], a[160:167], v19\n"
        "ds_read_b128 v[108:111], v5 offset:19456\n"
        "v_smfmac_f32_16x16x64_bf16 v[60:63], v[40:43], a[96:103], v19\n"
        "v_smfmac_f32_16x16x64_bf16 v[68:71], v[40:43], a[224:231], v19\n"
        "s_waitcnt lgkmcnt(10)\n"
        "v_smfmac_f32_16x16x64_bf16 v[56:59], v[44:47], a[40:47], v19\n"
        "v_smfmac_f32_16x16x64_bf16 v[64:67], v[44:47], a[168:175], v19\n"
        "v_smfmac_f32_16x16x64_bf16 v[60:63], v[44:47], a[104:111], v19\n"
        "v_smfmac_f32_16x16x64_bf16 v[68:71], v[44:47], a[232:239], v19\n"
        "s_waitcnt lgkmcnt(9)\n"
        "v_smfmac_f32_16x16x64_bf16 v[56:59], v[48:51], a[48:55], v19\n"
        "v_smfmac_f32_16x16x64_bf16 v[64:67], v[48:51], a[176:183], v19\n"
        "s_waitcnt lgkmcnt(8)\n"
        "v_smfmac_f32_16x16x64_bf16 v[56:59], v[52:55], a[56:63], v19\n"
        "v_smfmac_f32_16x16x64_bf16 v[64:67], v[52:55], a[184:191], v19\n"
        "v_smfmac_f32_16x16x64_bf16 v[60:63], v[48:51], a[112:119], v19\n"
        "ds_read_b128 v[24:27], v9 offset:12288\n"
        "v_smfmac_f32_16x16x64_bf16 v[68:71], v[48:51], a[240:247], v19\n"
        "ds_read_b128 v[28:31], v9 offset:16384\n"
        "v_smfmac_f32_16x16x64_bf16 v[60:63], v[52:55], a[120:127], v19\n"
        "s_mov_b32 vcc_lo, 0x33333333\n"
        "v_smfmac_f32_16x16x64_bf16 v[68:71], v[52:55], a[248:255], v19\n"
        "s_mov_b32 vcc_hi, 0x33333333\n"
        "v_add_f32 v56, v56, v57\n"
        "v_add_f32 v58, v58, v59\n"
        "v_add_f32 v64, v64, v65\n"
        "v_add_f32 v66, v66, v67\n"
        "v_cvt_pk_bf16_f32 v56, v56, v58\n"
        "v_cvt_pk_bf16_f32 v57, v64, v66\n"
        "ds_write_b64 v6, v[56:57]\n"
        "v_add_f32 v60, v60, v61\n"
        "v_add_f32 v62, v62, v63\n"
        "v_add_f32 v68, v68, v69\n"
        "v_add_f32 v70, v70, v71\n"
        "v_cvt_pk_bf16_f32 v60, v60, v62\n"
        "v_cvt_pk_bf16_f32 v61, v68, v70\n"
        "ds_write_b64 v6, v[60:61] offset:512\n"
        "v_smfmac_f32_16x16x64_bf16 v[72:75], v[32:35], v[112:119], v19\n"
        "v_smfmac_f32_16x16x64_bf16 v[76:79], v[32:35], v[120:127], v19\n"
        "s_waitcnt lgkmcnt(10)\n"
        "v_smfmac_f32_16x16x64_bf16 v[72:75], v[36:39], v[80:87], v19\n"
        "ds_read_b128 v[32:35], v9 offset:13312\n"
        "s_waitcnt lgkmcnt(9)\n"
        "v_smfmac_f32_16x16x64_bf16 v[76:79], v[36:39], v[88:95], v19\n"
        "ds_read_b128 v[36:39], v9 offset:17408\n"
        "s_cmp_eq_u32 s40, 63\n"
        "s_waitcnt lgkmcnt(2)\n"
        "s_barrier\n"
        "s_cbranch_scc1 LAST%=\n"
        "ds_read_b64_tr_b16 v[56:57], v7 offset:0\n"
        "ds_read_b64_tr_b16 v[58:59], v7 offset:512\n"
        "ds_read_b64_tr_b16 v[60:61], v7 offset:1024\n"
        "ds_read_b64_tr_b16 v[62:63], v7 offset:1536\n"
        "ds_read_b64_tr_b16 v[64:65], v7 offset:2048\n"
        "ds_read_b64_tr_b16 v[66:67], v7 offset:2560\n"
        "ds_read_b64_tr_b16 v[68:69], v7 offset:3072\n"
        "ds_read_b64_tr_b16 v[70:71], v7 offset:3584\n"
        "v_smfmac_f32_16x16x64_bf16 v[72:75], v[40:43], v[96:103], v19\n"
        "ds_read_b128 v[124:127], v9 offset:18432\n"
        "v_smfmac_f32_16x16x64_bf16 v[76:79], v[40:43], v[104:111], v19\n"
        "ds_read_b128 v[40:43], v9 offset:14336\n"
        "s_waitcnt lgkmcnt(8)\n"
        "v_mfma_f32_16x16x32_bf16 v[112:115], v[56:59], v[56:59], 0\n"
        "v_mfma_f32_16x16x32_bf16 v[116:119], v[24:27], v[56:59], 0\n"
        "ds_read_b128 v[24:27], v9 offset:15360\n"
        "v_mfma_f32_16x16x32_bf16 v[120:123], v[28:31], v[56:59], 0\n"
        "ds_read_b128 v[28:31], v9 offset:19456\n"
        "s_waitcnt lgkmcnt(8)\n"
        "v_mfma_f32_16x16x32_bf16 v[112:115], v[60:63], v[60:63], v[112:115]\n"
        "ds_read_b128 v[80:83], v5 offset:20480\n"
        "v_mfma_f32_16x16x32_bf16 v[116:119], v[32:35], v[60:63], v[116:119]\n"
        "ds_read_b128 v[84:87], v5 offset:21504\n"
        "v_mfma_f32_16x16x32_bf16 v[120:123], v[36:39], v[60:63], v[120:123]\n"
        "ds_read_b128 v[88:91], v5 offset:22528\n"
        "s_waitcnt lgkmcnt(9)\n"
        "v_mfma_f32_16x16x32_bf16 v[112:115], v[64:67], v[64:67], v[112:115]\n"
        "ds_read_b128 v[92:95], v5 offset:23552\n"
        "s_waitcnt lgkmcnt(6)\n"
        "v_mfma_f32_16x16x32_bf16 v[116:119], v[40:43], v[64:67], v[116:119]\n"
        "ds_read_b128 v[96:99], v5 offset:24576\n"
        "v_mfma_f32_16x16x32_bf16 v[120:123], v[124:127], v[64:67], v[120:123]\n"
        "ds_read_b128 v[100:103], v5 offset:25600\n"
        "v_mfma_f32_16x16x32_bf16 v[112:115], v[68:71], v[68:71], v[112:115]\n"
        "ds_read_b128 v[104:107], v5 offset:26624\n"
        "s_waitcnt lgkmcnt(8)\n"
        "v_mfma_f32_16x16x32_bf16 v[116:119], v[24:27], v[68:71], v[116:119]\n"
        "s_waitcnt lgkmcnt(7)\n"
        "v_mfma_f32_16x16x32_bf16 v[120:123], v[28:31], v[68:71], v[120:123]\n"
        "ds_read_b128 v[108:111], v5 offset:27648\n"
        "s_waitcnt lgkmcnt(6)\n"
        "v_smfmac_f32_16x16x64_bf16 v[72:75], v[44:47], v[80:87], v19\n"
        "ds_read_b128 v[80:83], v5 offset:28672\n"
        "ds_read_b128 v[84:87], v5 offset:29696\n"
        "s_waitcnt lgkmcnt(6)\n"
        "v_smfmac_f32_16x16x64_bf16 v[76:79], v[44:47], v[88:95], v19\n"
        "ds_read_b128 v[88:91], v5 offset:30720\n"
        "ds_read_b128 v[92:95], v5 offset:31744\n"
        "v_cndmask_b32_dpp v114, v115, v114, vcc quad_perm:[0,1,0,1] row_mask:0xf bank_mask:0xf\n"
        "v_cndmask_b32_dpp v116, v120, v116, vcc quad_perm:[0,1,0,1] row_mask:0xf bank_mask:0xf\n"
        "v_cndmask_b32_dpp v117, v121, v117, vcc quad_perm:[0,1,0,1] row_mask:0xf bank_mask:0xf\n"
        "v_cndmask_b32_dpp v118, v122, v118, vcc quad_perm:[0,1,0,1] row_mask:0xf bank_mask:0xf\n"
        "v_cndmask_b32_dpp v119, v123, v119, vcc quad_perm:[0,1,0,1] row_mask:0xf bank_mask:0xf\n"
        "v_max3_f32 v32, v114, v116, v117\n"
        "v_max3_f32 v32, v32, v118, v119\n"
        "s_waitcnt lgkmcnt(6)\n"
        "v_smfmac_f32_16x16x64_bf16 v[72:75], v[48:51], v[96:103], v19\n"
        "v_mov_b32 v60, 0\n"
        "v_max_f32_dpp v33, v32, v32 quad_perm:[2,3,0,1] row_mask:0xf bank_mask:0xf\n"
        "v_max_f32_dpp v32, v32, v32 quad_perm:[2,3,0,1] row_mask:0xf bank_mask:0xf\n"
        "s_waitcnt lgkmcnt(4)\n"
        "v_smfmac_f32_16x16x64_bf16 v[76:79], v[48:51], v[104:111], v19\n"
        "v_permlane16_swap_b32 v32, v33\n"
        "v_max_f32 v34, v32, v33\n"
        "v_max_f32 v35, v32, v33\n"
        "s_waitcnt lgkmcnt(2)\n"
        "v_smfmac_f32_16x16x64_bf16 v[72:75], v[52:55], v[80:87], v19\n"
        "s_waitcnt lgkmcnt(0)\n"
        "v_smfmac_f32_16x16x64_bf16 v[76:79], v[52:55], v[88:95], v19\n"
        "v_permlane32_swap_b32 v34, v35\n"
        "v_max_f32 v32, v34, v35\n"
        "v_mul_f32 v32, s52, v32\n"
        "v_fma_f32 v116, v116, s52, -v32\n"
        "ds_read_b128 v[24:27], v1 offset:20480\n"
        "v_fma_f32 v117, v117, s52, -v32\n"
        "v_fma_f32 v118, v118, s52, -v32\n"
        "ds_read_b128 v[28:31], v1 offset:21504\n"
        "v_fma_f32 v119, v119, s52, -v32\n"
        "v_fma_f32 v114, v114, s52, -v32\n"
        "v_exp_f32 v116, v116\n"
        "v_exp_f32 v117, v117\n"
        "v_exp_f32 v118, v118\n"
        "v_exp_f32 v119, v119\n"
        "v_exp_f32 v114, v114\n"
        "v_cvt_pk_bf16_f32 v56, v116, v117\n"
        "v_cvt_pk_bf16_f32 v57, v118, v119\n"
        "v_mov_b32_dpp v115, v114 quad_perm:[2,3,2,3] row_mask:0xf bank_mask:0xf\n"
        "v_mov_b32_dpp v58, v56 quad_perm:[2,3,2,3] row_mask:0xf bank_mask:0xf\n"
        "v_mov_b32_dpp v59, v57 quad_perm:[2,3,2,3] row_mask:0xf bank_mask:0xf\n"
        "v_cvt_pk_bf16_f32 v61, v114, v115\n"
        "v_add_f32 v34, v116, v117\n"
        "s_waitcnt lgkmcnt(1)\n"
        "v_mfma_f32_16x16x32_bf16 v[64:67], v[24:27], v[56:59], 0\n"
        "v_add_f32 v72, v72, v73\n"
        "v_add_f32 v74, v74, v75\n"
        "s_waitcnt lgkmcnt(0)\n"
        "v_mfma_f32_16x16x32_bf16 v[68:71], v[28:31], v[56:59], 0\n"
        "v_add_f32 v76, v76, v77\n"
        "v_add_f32 v78, v78, v79\n"
        "v_cvt_pk_bf16_f32 v21, v72, v74\n"
        "v_cvt_pk_bf16_f32 v23, v76, v78\n"
        "v_add_f32 v35, v118, v119\n"
        "v_add_f32 v34, v34, v114\n"
        "v_mfma_f32_16x16x16_bf16 v[64:67], v[20:21], v[60:61], v[64:67]\n"
        "v_add_f32 v34, v34, v35\n"
        "v_mov_b64 v[80:81], 0\n"
        "v_mfma_f32_16x16x16_bf16 v[68:71], v[22:23], v[60:61], v[68:71]\n"
        "v_mov_b64 v[82:83], 0\n"
        "v_add_f32_dpp v35, v34, v34 quad_perm:[2,3,0,1] row_mask:0xf bank_mask:0xf\n"
        "v_add_f32_dpp v34, v34, v34 quad_perm:[2,3,0,1] row_mask:0xf bank_mask:0xf\n"
        "v_mov_b64 v[84:85], 0\n"
        "v_mov_b64 v[86:87], 0\n"
        "v_permlane16_swap_b32 v34, v35\n"
        "v_add_f32 v36, v34, v35\n"
        "v_add_f32 v37, v34, v35\n"
        "v_mov_b64 v[88:89], 0\n"
        "v_mov_b64 v[90:91], 0\n"
        "v_permlane32_swap_b32 v36, v37\n"
        "v_add_f32 v34, v36, v37\n"
        "v_rcp_f32 v34, v34\n"
        "v_mov_b64 v[92:93], 0\n"
        "v_pk_mul_f32 v[64:65], v[64:65], v[34:35] op_sel_hi:[1,0]\n"
        "v_pk_mul_f32 v[66:67], v[66:67], v[34:35] op_sel_hi:[1,0]\n"
        "v_pk_mul_f32 v[68:69], v[68:69], v[34:35] op_sel_hi:[1,0]\n"
        "v_pk_mul_f32 v[70:71], v[70:71], v[34:35] op_sel_hi:[1,0]\n"
        "v_cvt_pk_bf16_f32 v64, v64, v65\n"
        "v_cvt_pk_bf16_f32 v65, v66, v67\n"
        "ds_write_b64 v11, v[64:65]\n"
        "v_cvt_pk_bf16_f32 v68, v68, v69\n"
        "v_cvt_pk_bf16_f32 v69, v70, v71\n"
        "ds_write_b64 v11, v[68:69] offset:256\n"
        "v_mov_b64 v[94:95], 0\n"
        "v_mov_b64 v[96:97], 0\n"
        "v_mov_b64 v[98:99], 0\n"
        "v_mov_b64 v[100:101], 0\n"
        "v_mov_b64 v[102:103], 0\n"
        "v_mov_b64 v[104:105], 0\n"
        "v_mov_b64 v[106:107], 0\n"
        "v_mov_b64 v[108:109], 0\n"
        "v_mov_b64 v[110:111], 0\n"
        "s_waitcnt lgkmcnt(0)\n"
        "s_barrier\n"
        "ds_read_b128 v[24:27], v9 offset:28672\n"
        "ds_read_b128 v[28:31], v9 offset:29696\n"
        "s_waitcnt lgkmcnt(1)\n"
        "v_smfmac_f32_16x16x64_bf16 v[80:83], v[24:27], v[128:135], v19\n"
        "v_smfmac_f32_16x16x64_bf16 v[84:87], v[24:27], v[144:151], v19\n"
        "s_waitcnt lgkmcnt(0)\n"
        "v_smfmac_f32_16x16x64_bf16 v[80:83], v[28:31], v[136:143], v19\n"
        "v_smfmac_f32_16x16x64_bf16 v[84:87], v[28:31], v[152:159], v19\n"
        "v_smfmac_f32_16x16x64_bf16 v[88:91], v[24:27], v[160:167], v19\n"
        "v_smfmac_f32_16x16x64_bf16 v[92:95], v[24:27], v[176:183], v19\n"
        "s_nop 4\n"
        "v_add_f32 v80, v80, v81\n"
        "v_add_f32 v81, v82, v83\n"
        "v_smfmac_f32_16x16x64_bf16 v[88:91], v[28:31], v[168:175], v19\n"
        "v_add_f32 v82, v84, v85\n"
        "v_add_f32 v83, v86, v87\n"
        "v_bfi_b32 v80, 1, s44, v80\n"
        "v_smfmac_f32_16x16x64_bf16 v[92:95], v[28:31], v[184:191], v19\n"
        "v_bfi_b32 v81, 1, s44, v81\n"
        "v_bfi_b32 v82, 1, s44, v82\n"
        "v_bfi_b32 v83, 1, s44, v83\n"
        "buffer_store_dwordx4 v[80:83], v13, s[36:39], 0 offen sc1\n"
        "v_smfmac_f32_16x16x64_bf16 v[96:99], v[24:27], v[192:199], v19\n"
        "v_smfmac_f32_16x16x64_bf16 v[100:103], v[24:27], v[208:215], v19\n"
        "v_add_f32 v88, v88, v89\n"
        "v_add_f32 v89, v90, v91\n"
        "v_smfmac_f32_16x16x64_bf16 v[96:99], v[28:31], v[200:207], v19\n"
        "v_add_f32 v90, v92, v93\n"
        "v_add_f32 v91, v94, v95\n"
        "v_bfi_b32 v88, 1, s44, v88\n"
        "v_smfmac_f32_16x16x64_bf16 v[100:103], v[28:31], v[216:223], v19\n"
        "v_bfi_b32 v89, 1, s44, v89\n"
        "v_bfi_b32 v90, 1, s44, v90\n"
        "v_bfi_b32 v91, 1, s44, v91\n"
        "buffer_store_dwordx4 v[88:91], v13, s[36:39], s45 offen sc1\n"
        "v_smfmac_f32_16x16x64_bf16 v[104:107], v[24:27], v[224:231], v19\n"
        "v_smfmac_f32_16x16x64_bf16 v[108:111], v[24:27], v[240:247], v19\n"
        "v_add_f32 v96, v96, v97\n"
        "v_add_f32 v97, v98, v99\n"
        "v_smfmac_f32_16x16x64_bf16 v[104:107], v[28:31], v[232:239], v19\n"
        "v_add_f32 v98, v100, v101\n"
        "v_add_f32 v99, v102, v103\n"
        "v_bfi_b32 v96, 1, s44, v96\n"
        "v_smfmac_f32_16x16x64_bf16 v[108:111], v[28:31], v[248:255], v19\n"
        "v_bfi_b32 v97, 1, s44, v97\n"
        "v_bfi_b32 v98, 1, s44, v98\n"
        "v_bfi_b32 v99, 1, s44, v99\n"
        "buffer_store_dwordx4 v[96:99], v13, s[36:39], s46 offen sc1\n"
        "v_add_f32 v104, v104, v105\n"
        "v_add_f32 v105, v106, v107\n"
        "s_nop 1\n"
        "v_add_f32 v106, v108, v109\n"
        "v_add_f32 v107, v110, v111\n"
        "v_bfi_b32 v104, 1, s44, v104\n"
        "v_bfi_b32 v105, 1, s44, v105\n"
        "v_bfi_b32 v106, 1, s44, v106\n"
        "v_bfi_b32 v107, 1, s44, v107\n"
        "buffer_store_dwordx4 v[104:107], v13, s[36:39], s47 offen sc1\n"
        "s_sleep 18\n"
        "ds_read_b64_tr_b16 v[32:33], v8\n"
        "ds_read_b64_tr_b16 v[34:35], v8 offset:512\n"
        "v_min_u32 v36, -2, v21\n"
        "v_min_u32 v37, -2, v23\n"
        "buffer_store_dwordx2 v[36:37], v15, s[36:39], s48 offen sc1\n"
        "s_waitcnt lgkmcnt(0)\n"
        "v_min_u32 v32, -2, v32\n"
        "v_min_u32 v33, -2, v33\n"
        "v_min_u32 v34, -2, v34\n"
        "v_min_u32 v35, -2, v35\n"
        "buffer_store_dwordx4 v[32:35], v14, s[36:39], s48 offen sc1\n"
        "s_waitcnt vmcnt(7)\n"
        "v_cmp_eq_u32 vcc, -1, v10\n"
        "s_mov_b32 m0, s54\n"
        "s_cbranch_vccnz GWAIT%=\n"
        "GOK%=:\n"
        "buffer_load_dwordx4 v16, s[36:39], s42 offen sc1 lds\n"
        "buffer_load_dwordx4 v16, s[36:39], s42 offen offset:1024 sc1 lds\n"
        "s_mov_b32 m0, s55\n"
        "s_nop 0\n"
        "buffer_load_dwordx4 v16, s[36:39], s43 offen sc1 lds\n"
        "buffer_load_dwordx4 v16, s[36:39], s43 offen offset:1024 sc1 lds\n"
        "s_add_u32 s40, s40, 1\n"
        "s_and_b32 s41, s40, 3\n"
        "s_lshl_b32 s41, s41, 16\n"
        "s_add_u32 s42, s42, 262144\n"
        "s_add_u32 s43, s43, 262144\n"
        "s_xor_b32 s44, s44, 1\n"
        "s_add_u32 s48, s48, 65536\n"
        "s_add_u32 s49, s49, 256\n"
        "s_add_u32 s50, s50, 512\n"
        "s_nop 1\n"
        "s_sleep 8\n"
        "s_branch LOOP%=\n"
        "GWAIT%=:\n"
        "s_mov_b32 s51, 0\n"
        "GSPIN%=:\n"
        "buffer_load_dword v10, v17, s[36:39], s49 offen sc1\n"
        "s_waitcnt vmcnt(0)\n"
        "v_cmp_eq_u32 vcc, -1, v10\n"
        "s_nop 4\n"
        "s_cbranch_vccz GOK%=\n"
        "s_add_u32 s51, s51, 1\n"
        "s_cmp_lt_u32 s51, 524288\n"
        "s_cbranch_scc1 GSPIN%=\n"
        "ABORT%=:\n"
        "s_waitcnt vmcnt(0) lgkmcnt(0)\n"
        "v_mov_b32 v32, 0\n"
        "v_mov_b32 v33, 0\n"
        "buffer_store_dword v32, v33, s[36:39], 0 offen sc1\n"
        "s_branch END%=\n"
        "LAST%=:\n"
        "s_waitcnt lgkmcnt(0)\n"
        "s_nop 7\n"
        "v_smfmac_f32_16x16x64_bf16 v[72:75], v[40:43], v[96:103], v19\n"
        "v_smfmac_f32_16x16x64_bf16 v[76:79], v[40:43], v[104:111], v19\n"
        "ds_read_b128 v[80:83], v5 offset:20480\n"
        "ds_read_b128 v[84:87], v5 offset:21504\n"
        "ds_read_b128 v[88:91], v5 offset:22528\n"
        "ds_read_b128 v[92:95], v5 offset:23552\n"
        "ds_read_b128 v[112:115], v5 offset:24576\n"
        "ds_read_b128 v[116:119], v5 offset:25600\n"
        "ds_read_b128 v[120:123], v5 offset:26624\n"
        "ds_read_b128 v[124:127], v5 offset:27648\n"
        "ds_read_b128 v[96:99], v5 offset:28672\n"
        "ds_read_b128 v[100:103], v5 offset:29696\n"
        "ds_read_b128 v[104:107], v5 offset:30720\n"
        "ds_read_b128 v[108:111], v5 offset:31744\n"
        "ds_read_b64_tr_b16 v[32:33], v8\n"
        "ds_read_b64_tr_b16 v[34:35], v8 offset:512\n"
        "s_waitcnt lgkmcnt(0)\n"
        "v_smfmac_f32_16x16x64_bf16 v[72:75], v[44:47], v[80:87], v19\n"
        "v_smfmac_f32_16x16x64_bf16 v[76:79], v[44:47], v[88:95], v19\n"
        "v_smfmac_f32_16x16x64_bf16 v[72:75], v[48:51], v[112:119], v19\n"
        "v_smfmac_f32_16x16x64_bf16 v[76:79], v[48:51], v[120:127], v19\n"
        "v_smfmac_f32_16x16x64_bf16 v[72:75], v[52:55], v[96:103], v19\n"
        "v_smfmac_f32_16x16x64_bf16 v[76:79], v[52:55], v[104:111], v19\n"
        "v_min_u32 v32, -2, v32\n"
        "v_min_u32 v33, -2, v33\n"
        "v_min_u32 v34, -2, v34\n"
        "v_min_u32 v35, -2, v35\n"
        "buffer_store_dwordx4 v[32:35], v14, s[36:39], s48 offen sc1\n"
        "s_nop 1\n"
        "v_add_f32 v72, v72, v73\n"
        "v_add_f32 v74, v74, v75\n"
        "v_add_f32 v76, v76, v77\n"
        "v_add_f32 v78, v78, v79\n"
        "v_cvt_pk_bf16_f32 v36, v72, v74\n"
        "v_cvt_pk_bf16_f32 v37, v76, v78\n"
        "v_min_u32 v36, -2, v36\n"
        "v_min_u32 v37, -2, v37\n"
        "buffer_store_dwordx2 v[36:37], v15, s[36:39], s48 offen sc1\n"
        "s_add_u32 s50, s50, 512\n"
        "v_mov_b32 v12, 1\n"
        "s_waitcnt vmcnt(0)\n"
        "buffer_store_dword v12, v18, s[36:39], s50 offen sc1\n"
        "END%=:\n"
        "s_waitcnt vmcnt(0) lgkmcnt(0)\n"
        : "+{v[0:15]}"(P0), "+{v[16:23]}"(P1), "+{s56}"(tsb)
        : "{a[0:31]}"(a0), "{a[32:63]}"(a1), "{a[64:95]}"(a2), "{a[96:127]}"(a3), "{a[128:159]}"(a4), "{a[160:191]}"(a5), "{a[192:223]}"(a6), "{a[224:255]}"(a7),
          "{v[128:159]}"(o0), "{v[160:191]}"(o1), "{v[192:223]}"(o2), "{v[224:255]}"(o3), "{s[36:39]}"(ws), "{s54}"(mk), "{s55}"(mv)
        : "memory", "vcc", "scc", "v24", "v25", "v26", "v27", "v28", "v29", "v30", "v31", "v32", "v33", "v34", "v35", "v36", "v37", "v38", "v39", "v40", "v41", "v42", "v43", "v44", "v45", "v46", "v47", "v48", "v49", "v50", "v51", "v52", "v53", "v54", "v55", "v56", "v57", "v58", "v59", "v60", "v61", "v62", "v63", "v64", "v65", "v66", "v67", "v68", "v69", "v70", "v71", "v72", "v73", "v74", "v75", "v76", "v77", "v78", "v79", "v80", "v81", "v82", "v83", "v84", "v85", "v86", "v87", "v88", "v89", "v90", "v91", "v92", "v93", "v94", "v95", "v96", "v97", "v98", "v99", "v100", "v101", "v102", "v103", "v104", "v105", "v106", "v107", "v108", "v109", "v110", "v111", "v112", "v113", "v114", "v115", "v116", "v117", "v118", "v119", "v120", "v121", "v122", "v123", "v124", "v125", "v126", "v127", "s40", "s41", "s42", "s43", "s44", "s45", "s46", "s47", "s48", "s49", "s50", "s51", "s52", "s53", "s60", "s61", "s62", "s63", "s64", "s65", "s66", "s67", "s68", "s69", "s70", "s71", "s72", "s73", "s74", "s75", "s76", "s77");
}

DEV void h_role(const Args& A, ldsp lds, int h, int wave, int lane) {
    const rsrc_t ws = mkrsrc(A.ws, WS_END);
    const int g = lane >> 4, c16 = lane & 15, q = (lane >> 2) & 3, p = lane & 3;
    v16bf Wqk[4][8];
    v16bf Wo[8][2];
    {
        ldsp my = lds + L_WV + wave * 32768;
        const unsigned myb = L_WV + wave * 32768, exb = L_MEM + wave * 8192;
        auto slot = [&](int c) { const int sl = (c + 3) % 5; return sl < 4 ? myb + sl * 8192 : exb; };
        const int r4 = lane >> 4, x = ((lane >> 1) & 7) ^ ((r4 & 1) | ((r4 >> 1) << 2));
        const unsigned vqkv = (4 * r4 + (x >> 1)) * 8192 + h * 256 + wave * 64 + (2 * (x & 1) + (lane & 1)) * 16;
        const unsigned vo = (h * 128 + r4) * 1024 + wave * 256 + ((((lane >> 1) & 7) ^ (r4 & 1)) * 32) + (lane & 1) * 16;
        const rsrc_t rq = mkrsrc(A.wq, 512 * 4096 * 2), rk = mkrsrc(A.wk, 512 * 4096 * 2), rv = mkrsrc(A.wv, 512 * 4096 * 2), ro = mkrsrc(A.wo, 4096 * 512 * 2);
        auto issue = [&](int c) {
            const int m = c >> 2, cc = c & 3;
#pragma unroll
            for (int i = 0; i < 8; ++i) {
                const int sl = (c + 3) % 5, lo = (sl < 4 ? sl * 8192 : 0) + i * 1024;
                if (m == 2) dma16k(ro, vo ^ ((((i & 1) << 1) | (((i >> 1) & 1) << 2)) * 32), (cc * 32 + i * 4) * 1024, sl < 4 ? myb : exb, lo);
                else dma16k(m == 0 ? rq : m == 1 ? rk : rv, vqkv + (cc ^ wave) * (8 * 131072), i * 131072, sl < 4 ? myb : exb, lo);
            }
        };
        int ra[2][2];
#pragma unroll
        for (int rd = 0; rd < 2; ++rd)
#pragma unroll
            for (int t = 0; t < 2; ++t) {
                const int row = 16 * (g & 1) + 8 * (g >> 1) + 4 * (q >> 1) + 2 * rd + (q & 1);
                const int sig = ((row >> 2) & 1) | (((row >> 3) & 1) << 2);
                ra[rd][t] = (row >> 2) * 256 + ((((row & 3) * 2 + t) ^ sig) * 32) + p * 8;
            }
        auto frag = [&](ldsp b, int t) {
            const v2u r0 = ltr(b + ra[0][t]), r1 = ltr(b + ra[1][t]), r2 = ltr(b + 2048 + ra[0][t]), r3 = ltr(b + 2048 + ra[1][t]);
            return as16(v4u{r0.x, r0.y, r1.x, r1.y}, v4u{r2.x, r2.y, r3.x, r3.y});
        };
        v4u wo4[8][4];
        unsigned hbv = 0, hb0 = 0;
        if (wave == 0) hbv = hb_issue(ws);
#pragma unroll
        for (int c = 0; c < 5; ++c) issue(c);
#pragma unroll
        for (int c = 0; c < 16; ++c) {
            const int m = c >> 2, cc = c & 3, left = (15 - c < 4 ? 15 - c : 4) * 8;
            wait_vm(left);
            if (c == 0 && wave == 0) hb0 = hb_get(hbv);
            ldsp b = lds + slot(c);
            if (m < 2) {
#pragma unroll
                for (int k2 = 0; k2 < 2; ++k2)
#pragma unroll
                    for (int t = 0; t < 2; ++t) { Wqk[2 * m + t][2 * cc + k2] = frag(b + k2 * 4096, t); asm volatile("" : "+a"(Wqk[2 * m + t][2 * cc + k2])); }
                wait_lgkm0();
            } else if (m == 2) {
#pragma unroll
                for (int ct = 0; ct < 8; ++ct) {
                    v2u r[2];
#pragma unroll
                    for (int rd = 0; rd < 2; ++rd) {
                        const int row = 16 * (g >> 1) + 4 * (g & 1) + (q & 1) + 8 * (q >> 1) + 2 * rd;
                        const int f = (row & 1) | (((row >> 2) & 1) << 1) | (((row >> 3) & 1) << 2);
                        r[rd] = ltr(b + row * 256 + ((ct ^ f) * 32) + p * 8);
                    }
                    wo4[ct][cc] = v4u{r[0].x, r[0].y, r[1].x, r[1].y};
                    asm volatile("" : "+v"(wo4[ct][cc]));
                }
                wait_lgkm0();
            } else {
                v16bf vt[2][2];
#pragma unroll
                for (int k2 = 0; k2 < 2; ++k2)
#pragma unroll
                    for (int t = 0; t < 2; ++t) { vt[k2][t] = frag(b + k2 * 4096, t); asm volatile("" : "+v"(vt[k2][t])); }
                wait_lgkm0();
#pragma unroll
                for (int k2 = 0; k2 < 2; ++k2)
#pragma unroll
                    for (int t = 0; t < 2; ++t) {
                        const v8u u = __builtin_bit_cast(v8u, vt[k2][t]);
                        lw128(b + (k2 * 2 + t) * 2048 + lane * 16, v4u{u[0], u[1], u[2], u[3]});
                        lw128(b + (k2 * 2 + t) * 2048 + 1024 + lane * 16, v4u{u[4], u[5], u[6], u[7]});
                    }
            }
            if (c + 5 < 16) issue(c + 5);
            if (c == 10 && wave == 0) hbv = hb_issue(ws);
        }

        if (wave == 0) hb_finish(ws, hb0, hb_get(hbv), lane);

#pragma unroll
        for (int ct = 0; ct < 8; ++ct)
#pragma unroll
            for (int kb = 0; kb < 2; ++kb) Wo[ct][kb] = as16(wo4[ct][2 * kb], wo4[ct][2 * kb + 1]);
        bar();

    }
    asm volatile("v_mbcnt_lo_u32_b32 %0, -1, 0\n\tv_mbcnt_hi_u32_b32 %0, -1, %0" : "=v"(lane));
    h_run(A, lds, h, wave, lane, Wqk, Wo);
}

DEV void r_role(const Args& A, int jw, int lane) {
    const rsrc_t ws = mkrsrc(A.ws, WS_END);
    const int row = 2 * ((lane >> 2) & 1) + ((lane >> 4) & 1), ct = lane >> 5, cp = (lane >> 1) & 1, copy = ((lane >> 3) & 1) * 2 + (lane & 1);
    const int col = (jw >> 3) * 32 + ((jw >> 1) & 3) * 4 + ct * 16 + cp * 2;
    const unsigned d0 = min(*(const unsigned*)(A.bmem + ((4 * (jw & 1) + row) * 512 + col) * 2), 0xFFFFFFFEu);
    unsigned par;

    if (!get_par(ws, lane, par)) return;

    const unsigned a_in = WS_PART + (jw >> 1) * 16384 + par * 8192 + (jw & 1) * 4096 + lane * 16;
    const unsigned a_out = WS_MEMT + copy * 16384 + par * 8192 + jw * 64 + row * 16 + ct * 8 + cp * 4;
    const v4u pv = {POISON, POISON, POISON, POISON};
#pragma unroll
    for (int u = 0; u < 4; ++u) st128c(ws, a_in + u * 1024, pv);
#pragma unroll
    for (int u = 1; u < 4; ++u) st32c(ws, a_out + u * 65536, POISON);
#pragma unroll
    for (int u = DP_FIRST; u < NSEG; ++u) st128c(ws, (jw < 64 ? WS_KM : WS_VM - 65536) + u * 65536 + jw * 1024 + lane * 16, pv);
    wait_vm0();

    st32c(ws, a_out, d0);
    v4u P = {a_in, a_out, lane == 0 ? 0u : 0x80000000u, 0u};
    unsigned tsb = 0;
    asm volatile(
        "s_mov_b32 s40, 0\n"
        "s_mov_b32 s41, 65536\n"
        "s_mov_b32 s42, 196608\n"
        "v_mov_b32 v4, -1\n"
        "v_mov_b32 v5, -1\n"
        "v_mov_b32 v6, -1\n"
        "v_mov_b32 v7, -1\n"
        "LOOP%=:\n"
        "s_waitcnt vmcnt(0)\n"
        "s_mov_b32 s43, 0\n"
        "buffer_load_dwordx4 v[16:19], v0, s[36:39], 0 offen offset:0 sc1\n"
        "buffer_load_dwordx4 v[20:23], v0, s[36:39], 0 offen offset:1024 sc1\n"
        "buffer_load_dwordx4 v[24:27], v0, s[36:39], 0 offen offset:2048 sc1\n"
        "buffer_load_dwordx4 v[28:31], v0, s[36:39], 0 offen offset:3072 sc1\n"
        "P0_%=:\n"
        "s_waitcnt vmcnt(3)\n"
        "v_bitop3_b32 v8, v16, v17, v18 bitop3:0xfe\n"
        "s_waitcnt vmcnt(2)\n"
        "v_bitop3_b32 v9, v19, v20, v21 bitop3:0xfe\n"
        "v_bitop3_b32 v8, v8, v22, v23 bitop3:0xfe\n"
        "v_pk_add_f32 v[12:13], v[16:17], v[20:21]\n"
        "v_pk_add_f32 v[14:15], v[18:19], v[22:23]\n"
        "s_waitcnt vmcnt(1)\n"
        "v_bitop3_b32 v9, v9, v24, v25 bitop3:0xfe\n"
        "v_bitop3_b32 v8, v8, v26, v27 bitop3:0xfe\n"
        "v_pk_add_f32 v[12:13], v[12:13], v[24:25]\n"
        "v_pk_add_f32 v[14:15], v[14:15], v[26:27]\n"
        "s_waitcnt vmcnt(0)\n"
        "v_bitop3_b32 v9, v9, v28, v29 bitop3:0xfe\n"
        "v_bitop3_b32 v8, v8, v30, v31 bitop3:0xfe\n"
        "v_bitop3_b32 v8, v8, v9, 1 bitop3:0xa8\n"
        "v_cmp_ne_u32 vcc, 0, v8\n"
        "v_pk_add_f32 v[12:13], v[12:13], v[28:29]\n"
        "v_pk_add_f32 v[14:15], v[14:15], v[30:31]\n"
        "s_cbranch_vccz OK0_%=\n"
        "buffer_load_dwordx4 v[16:19], v0, s[36:39], 0 offen offset:0 sc1\n"
        "buffer_load_dwordx4 v[20:23], v0, s[36:39], 0 offen offset:1024 sc1\n"
        "buffer_load_dwordx4 v[24:27], v0, s[36:39], 0 offen offset:2048 sc1\n"
        "buffer_load_dwordx4 v[28:31], v0, s[36:39], 0 offen offset:3072 sc1\n"
        "s_add_u32 s43, s43, 1\n"
        "s_cmp_lt_u32 s43, 524288\n"
        "s_cbranch_scc1 P0_%=\n"
        "s_branch ABORT%=\n"
        "OK0_%=:\n"
        "v_permlane32_swap_b32 v12, v14\n"
        "v_permlane32_swap_b32 v13, v15\n"
        "v_add_f32 v12, v12, v14\n"
        "v_add_f32 v13, v13, v15\n"
        "s_nop 1\n"
        "v_permlane16_swap_b32 v12, v13\n"
        "v_add_f32 v12, v12, v13\n"
        "s_nop 1\n"
        "v_add_f32_dpp v12, v12, v12 row_ror:8 row_mask:0xf bank_mask:0xf\n"
        "s_nop 1\n"
        "v_mov_b32_dpp v13, v12 quad_perm:[0,0,2,2] row_mask:0xf bank_mask:0xf\n"
        "v_mov_b32_dpp v14, v12 quad_perm:[1,1,3,3] row_mask:0xf bank_mask:0xf\n"
        "v_cvt_pk_bf16_f32 v12, v13, v14\n"
        "v_min_u32 v12, -2, v12\n"
        "buffer_store_dword v12, v1, s[36:39], s41 offen sc1\n"
        "s_add_u32 s40, s40, 1\n"
        "s_cmp_eq_u32 s40, 63\n"
        "s_cbranch_scc1 END%=\n"
        "s_add_u32 s41, s41, 65536\n"
        "s_and_b32 s41, s41, 0x30000\n"
        "s_add_u32 s42, s42, 65536\n"
        "s_and_b32 s42, s42, 0x30000\n"
        "s_sleep 20\n"
        "buffer_store_dword v4, v1, s[36:39], s42 offen sc1\n"
        "s_sleep 46\n"
        "s_waitcnt vmcnt(0)\n"
        "s_mov_b32 s43, 0\n"
        "buffer_load_dwordx4 v[16:19], v0, s[36:39], 0 offen offset:0 sc1\n"
        "buffer_load_dwordx4 v[20:23], v0, s[36:39], 0 offen offset:1024 sc1\n"
        "buffer_load_dwordx4 v[24:27], v0, s[36:39], 0 offen offset:2048 sc1\n"
        "buffer_load_dwordx4 v[28:31], v0, s[36:39], 0 offen offset:3072 sc1\n"
        "P1_%=:\n"
        "s_waitcnt vmcnt(3)\n"
        "v_bitop3_b32 v8, v16, v17, v18 bitop3:0x80\n"
        "s_waitcnt vmcnt(2)\n"
        "v_bitop3_b32 v9, v19, v20, v21 bitop3:0x80\n"
        "v_bitop3_b32 v8, v8, v22, v23 bitop3:0x80\n"
        "v_pk_add_f32 v[12:13], v[16:17], v[20:21]\n"
        "v_pk_add_f32 v[14:15], v[18:19], v[22:23]\n"
        "s_waitcnt vmcnt(1)\n"
        "v_bitop3_b32 v9, v9, v24, v25 bitop3:0x80\n"
        "v_bitop3_b32 v8, v8, v26, v27 bitop3:0x80\n"
        "v_pk_add_f32 v[12:13], v[12:13], v[24:25]\n"
        "v_pk_add_f32 v[14:15], v[14:15], v[26:27]\n"
        "s_waitcnt vmcnt(0)\n"
        "v_bitop3_b32 v9, v9, v28, v29 bitop3:0x80\n"
        "v_bitop3_b32 v8, v8, v30, v31 bitop3:0x80\n"
        "v_bitop3_b32 v8, v8, v9, 1 bitop3:0x80\n"
        "v_cmp_eq_u32 vcc, 0, v8\n"
        "v_pk_add_f32 v[12:13], v[12:13], v[28:29]\n"
        "v_pk_add_f32 v[14:15], v[14:15], v[30:31]\n"
        "s_cbranch_vccz OK1_%=\n"
        "buffer_load_dwordx4 v[16:19], v0, s[36:39], 0 offen offset:0 sc1\n"
        "buffer_load_dwordx4 v[20:23], v0, s[36:39], 0 offen offset:1024 sc1\n"
        "buffer_load_dwordx4 v[24:27], v0, s[36:39], 0 offen offset:2048 sc1\n"
        "buffer_load_dwordx4 v[28:31], v0, s[36:39], 0 offen offset:3072 sc1\n"
        "s_add_u32 s43, s43, 1\n"
        "s_cmp_lt_u32 s43, 524288\n"
        "s_cbranch_scc1 P1_%=\n"
        "s_branch ABORT%=\n"
        "OK1_%=:\n"
        "v_permlane32_swap_b32 v12, v14\n"
        "v_permlane32_swap_b32 v13, v15\n"
        "v_add_f32 v12, v12, v14\n"
        "v_add_f32 v13, v13, v15\n"
        "s_nop 1\n"
        "v_permlane16_swap_b32 v12, v13\n"
        "v_add_f32 v12, v12, v13\n"
        "s_nop 1\n"
        "v_add_f32_dpp v12, v12, v12 row_ror:8 row_mask:0xf bank_mask:0xf\n"
        "s_nop 1\n"
        "v_mov_b32_dpp v13, v12 quad_perm:[0,0,2,2] row_mask:0xf bank_mask:0xf\n"
        "v_mov_b32_dpp v14, v12 quad_perm:[1,1,3,3] row_mask:0xf bank_mask:0xf\n"
        "v_cvt_pk_bf16_f32 v12, v13, v14\n"
        "v_min_u32 v12, -2, v12\n"
        "buffer_store_dword v12, v1, s[36:39], s41 offen sc1\n"
        "s_add_u32 s40, s40, 1\n"
        "s_cmp_eq_u32 s40, 63\n"
        "s_cbranch_scc1 END%=\n"
        "s_add_u32 s41, s41, 65536\n"
        "s_and_b32 s41, s41, 0x30000\n"
        "s_add_u32 s42, s42, 65536\n"
        "s_and_b32 s42, s42, 0x30000\n"
        "s_sleep 20\n"
        "buffer_store_dword v4, v1, s[36:39], s42 offen sc1\n"
        "s_sleep 46\n"
        "s_branch LOOP%=\n"
        "ABORT%=:\n"
        "s_waitcnt vmcnt(0)\n"
        "v_mov_b32 v8, 0\n"
        "v_mov_b32 v9, 0\n"
        "buffer_store_dword v8, v9, s[36:39], 0 offen sc1\n"
        "END%=:\n"
        "s_waitcnt vmcnt(0) lgkmcnt(0)\n"
        : "+{v[0:3]}"(P), "+{s56}"(tsb)
        : "{s[36:39]}"(ws)
        : "memory", "vcc", "scc", "v4", "v5", "v6", "v7", "v8", "v9", "v10", "v11", "v12", "v13", "v14", "v15", "v16", "v17", "v18", "v19", "v20", "v21", "v22", "v23", "v24", "v25", "v26", "v27", "v28", "v29", "v30", "v31", "v32", "v33", "v34", "v35", "v36", "v37", "v38", "v39", "v40", "v41", "v42", "v43", "v44", "v45", "v46", "v47", "v48", "v49", "v50", "v51", "v52", "v53", "v54", "v55", "v56", "v57", "v58", "v59", "v60", "v61", "v62", "v63", "v64", "v65", "v66", "v67", "v68", "v69", "s40", "s41", "s42", "s43", "s60", "s61", "s62", "s63", "s64", "s65");
}

DEV void x_role(const Args& A, int h, int wave, int lane) {
    const rsrc_t ws = mkrsrc(A.ws, WS_END);
    const int g = lane >> 4, c16 = lane & 15, qt = wave & 1;
    const float ninf = -__builtin_inff();
    if (wave >> 1) {
#pragma unroll
        for (int u = 0; u < 4; ++u) st128c(ws, WS_OX + ((NSEG - 1) * 32 + h) * 8192 + (qt * 4 + u) * 1024 + lane * 16, v4u{POISON, POISON, POISON, POISON});
    }
    for (int s = wave >> 1; s < NSEG; s += 2) {

        if (!wait_flag(ws, WS_GF + (s * 64 + ((lane & 1) ? vflag(h) : h)) * 4, true)) return;
        const unsigned b = (s * 32 + h) * 8192 + lane * 16;
        v4u qf[4], kf[8], vf[8];
#pragma unroll
        for (int i = 0; i < 4; ++i) qf[i] = ld128c(ws, WS_QX + b + (qt * 4 + i) * 1024);
#pragma unroll
        for (int i = 0; i < 8; ++i) kf[i] = ld128c(ws, WS_KX + b + i * 1024);
#pragma unroll
        for (int i = 0; i < 8; ++i) vf[i] = ld128c(ws, WS_VX + b + i * 1024);

        const bool last = s >= DP_FIRST;
        if (!last && !wait_flag(ws, WS_HF + (((s + 1) * 32 + h) * 4 + (lane & 3)) * 4, s < DP_FIRST - X_TIGHT)) return;

        const unsigned bm = (s * 32 + h) * 2048;
        v4u km[4]; v2u vm[4];
        for (unsigned spins = 0;;) {
            cfence();
#pragma unroll
            for (int i = 0; i < 4; ++i) km[i] = ld128c(ws, WS_KM + bm + i * 512 + g * 128 + (2 * (c16 >> 2) + (c16 & 1)) * 16);
#pragma unroll
            for (int i = 0; i < 4; ++i) vm[i] = __builtin_amdgcn_raw_buffer_load_b64(ws, WS_VM + bm + i * 512 + lane * 8, 0, SC1);
            if (!last) break;
            unsigned m = 0;
#pragma unroll
            for (int i = 0; i < 4; ++i) m = max(max(umax4(km[i]), m), max(vm[i].x, vm[i].y));
            if (__all(m != POISON)) break;
            if (spin_fail(ws, spins)) return;
        }
        v4f s0 = {0, 0, 0, 0}, s1 = {0, 0, 0, 0}, sm = {0, 0, 0, 0};
#pragma unroll
        for (int ks = 0; ks < 4; ++ks) {
            s0 = mfma32(as8(kf[ks]), as8(qf[ks]), s0);
            s1 = mfma32(as8(kf[4 + ks]), as8(qf[ks]), s1);
        }
#pragma unroll
        for (int ks = 0; ks < 4; ++ks) sm = mfma32(as8(km[ks]), as8(qf[ks]), sm);
        const int qx = 16 * qt + c16;
        float mx = ninf;
#pragma unroll
        for (int r = 0; r < 4; ++r) {
            sm[r] = r < 2 ? sm[r] * SM_C : ninf;
            s0[r] = (4 * g + r <= qx) ? s0[r] * SM_C : MASK_T;
            s1[r] = (16 + 4 * g + r <= qx) ? s1[r] * SM_C : MASK_T;
            mx = fmaxf(mx, fmaxf(sm[r], fmaxf(s0[r], s1[r])));
        }
        mx = xmax_g(mx);
        float sum = 0.f;
#pragma unroll
        for (int r = 0; r < 4; ++r) {
            sm[r] = r < 2 ? __builtin_amdgcn_exp2f(sm[r] - mx) : 0.f;
            s0[r] = __builtin_amdgcn_exp2f(s0[r] - mx);
            s1[r] = __builtin_amdgcn_exp2f(s1[r] - mx);
            sum += (sm[r] + s0[r]) + s1[r];
        }
        const v8bf px = as8(v4u{pk2(s0[0], s0[1]), pk2(s0[2], s0[3]), pk2(s1[0], s1[1]), pk2(s1[2], s1[3])});
        const v2u pm = {pk2(sm[0], sm[1]), 0u};
        const float rs = __builtin_amdgcn_rcpf(xsum_g(sum));
#pragma unroll
        for (int u = 0; u < 4; ++u) {
            v4f o0 = mfma32(as8(vf[2 * u]), px, v4f{0, 0, 0, 0}), o1 = mfma32(as8(vf[2 * u + 1]), px, v4f{0, 0, 0, 0});
            o0 = mfma16(v2u{vm[u].x, 0u}, pm, o0);
            o1 = mfma16(v2u{vm[u].y, 0u}, pm, o1);
            const v4u o = {min(pk2(o0[0] * rs, o0[1] * rs), POISON - 1), min(pk2(o0[2] * rs, o0[3] * rs), POISON - 1),
                           min(pk2(o1[0] * rs, o1[1] * rs), POISON - 1), min(pk2(o1[2] * rs, o1[3] * rs), POISON - 1)};
            st128c(ws, WS_OX + (s * 32 + h) * 8192 + (qt * 4 + u) * 1024 + lane * 16, o);
        }
        wait_vm0();
        if (lane == 0) st32c(ws, WS_XF + ((s * 2 + qt) * 32 + h) * 4, 1u);

    }
}

DEV void o_role(const Args& A, ldsp lds, int set, int cg, int wave, int lane) {
    const rsrc_t ws = mkrsrc(A.ws, WS_END);
    const rsrc_t wor = mkrsrc(A.wo, 4096 * 512 * 2);
    const int g = lane >> 4, c16 = lane & 15, q = (lane >> 2) & 3, p = lane & 3;
    const int qt = set & 1;
    v8bf W[8][4][2];
    if (!wait_flag(ws, WS_GF + (lane & 31) * 4, true)) return;
    {
        ldsp my = lds + wave * 8192;
        const int wrow = lane >> 2, wch = lane & 3;
#pragma unroll
        for (int hh = 0; hh < 8; ++hh) {
            v4u st[8];
#pragma unroll
            for (int i = 0; i < 8; ++i) st[i] = ld128(wor, ((wave * 8 + hh) * 128 + i * 16 + wrow) * 1024 + cg * 64 + wch * 16);
            wait_lgkm0();
#pragma unroll
            for (int i = 0; i < 8; ++i) {
                const int row = i * 16 + wrow;
                lw128(my + row * 64 + ((((wch >> 1) ^ ((row >> 2) & 1))) * 32) + (wch & 1) * 16, st[i]);
            }
            wait_lgkm0();
#pragma unroll
            for (int u = 0; u < 4; ++u)
#pragma unroll
                for (int ct = 0; ct < 2; ++ct) {
                    ldsp b = my + (32 * u + 4 * g + q) * 64 + ((ct ^ (g & 1)) * 32) + p * 8;
                    W[hh][u][ct] = as8(ltr(b), ltr(b + 1024));
                    asm volatile("" : "+a"(W[hh][u][ct]));
                }
        }
    }
    bar();
    ldsp red = lds + 32768;
    for (int s = set >> 1; s < NSEG; s += 2) {

        const bool last = s == NSEG - 1;
        if (!wait_flag(ws, WS_XF + (((last ? s - 1 : s) * 2 + qt) * 32 + wave * 8 + (lane & 7)) * 4, s < NSEG - XO_LAST)) return;

        v4u of[8][4];
        const unsigned ob = WS_OX + (s * 32 + wave * 8) * 8192 + qt * 4096 + lane * 16;
        for (unsigned spins = 0;;) {
            cfence();
            unsigned m = 0;
#pragma unroll
            for (int hh = 0; hh < 8; ++hh) { of[hh][3] = ld128c(ws, ob + hh * 8192 + 3072); }
            if (!last) break;
#pragma unroll
            for (int hh = 0; hh < 8; ++hh) m = max(m, umax4(of[hh][3]));
            if (__all(m != POISON)) break;
            if (spin_fail(ws, spins)) return;
        }
        for (unsigned spins = 0;;) {
            cfence();
            unsigned m = 0;
#pragma unroll
            for (int hh = 0; hh < 8; ++hh)
#pragma unroll
                for (int u = 0; u < 3; ++u) of[hh][u] = ld128c(ws, ob + hh * 8192 + u * 1024);
            if (!last) break;
#pragma unroll
            for (int hh = 0; hh < 8; ++hh)
#pragma unroll
                for (int u = 0; u < 3; ++u) m = max(m, umax4(of[hh][u]));
            if (__all(m != POISON)) break;
            if (spin_fail(ws, spins)) return;
        }
        v4f c0 = {0, 0, 0, 0}, c1 = {0, 0, 0, 0};
#pragma unroll
        for (int hh = 0; hh < 8; ++hh)
#pragma unroll
            for (int u = 0; u < 4; ++u) {
                asm volatile("" : "+a"(W[hh][u][0])); asm volatile("" : "+a"(W[hh][u][1]));
                c0 = mfma32(W[hh][u][0], as8(of[hh][u]), c0);
                c1 = mfma32(W[hh][u][1], as8(of[hh][u]), c1);
            }
        ldsp rb = red + ((s >> 1) & 1) * 16384;
        *(__attribute__((address_space(3))) v4f*)(rb + wave * 2304 + c16 * 144 + g * 16) = c0;
        *(__attribute__((address_space(3))) v4f*)(rb + wave * 2304 + c16 * 144 + 64 + g * 16) = c1;
        bar();
        {
            const int tok = threadIdx.x >> 4, cp = threadIdx.x & 15;
            float a0 = 0.f, a1 = 0.f;
#pragma unroll
            for (int w = 0; w < 4; ++w) {
                const v2u t = lr64(rb + w * 2304 + tok * 144 + cp * 8);
                a0 += __uint_as_float(t.x); a1 += __uint_as_float(t.y);
            }
            *(unsigned*)(A.out + (size_t)(s * 32 + qt * 16 + tok) * 2048 + 1024 + cg * 64 + cp * 4) = pk2(a0, a1);
        }

    }
}

constexpr int A_PS = 1568, A_UC = 4 * A_PS;
constexpr int A_CS = 4160, A_WW = 4 * A_CS;
constexpr int A_YS = 1056, A_YC = 4 * A_YS;
constexpr int A_U = 0, A_W = A_U + 8 * A_UC, A_Y = A_W + 4 * A_WW;
DEV float bfsel(const v4u& v, int c) { return (c & 1) ? bf_hi(v[c >> 1]) : bf_lo(v[c >> 1]); }
DEV void a_role(const Args& A, ldsp lds, int ai, int wave, int lane) {
    const int tid = threadIdx.x;
    const rsrc_t kr = mkrsrc(A.ak, 2048 * 1024), vr = mkrsrc(A.av, 2048 * 1024), qr = mkrsrc(A.aq, 2048 * 1024);
    const rsrc_t sr = mkrsrc(A.assm, 512 * 4096);
    for (int i = tid; i < 8 * 4 * 32; i += 256) lw128(lds + A_U + (i >> 7) * A_UC + ((i >> 5) & 3) * A_PS + (i & 31) * 16, v4u{0, 0, 0, 0});
    if (lane < 16) lw128(lds + A_W + wave * A_WW + (lane >> 2) * A_CS + (lane & 3) * 16, v4u{0, 0, 0, 0});
    const int G = lane >> 4, q = (lane >> 2) & 3, p = lane & 3, kg = lane >> 5, tb = lane & 31;
    if (!wait_flag(mkrsrc(A.ws, WS_END), WS_GF + (lane & 31) * 4, true)) return;
    for (int grp = 0; grp < 4; ++grp) {
        const int ch0 = ai * 32 + grp * 8;

        {
            v4u kk[11], vv[8];
#pragma unroll
            for (int i = 0; i < 11; ++i) {
                const int row = 8 * tid - 3 + i;
                kk[i] = ld128(kr, (unsigned)(row < 0 ? 0 : row) * 1024 + ch0 * 2);
                if (i < 3 && row < 0) kk[i] = v4u{0, 0, 0, 0};
            }
#pragma unroll
            for (int i = 0; i < 8; ++i) vv[i] = ld128(vr, (8 * tid + i) * 1024 + ch0 * 2);
            const unsigned short* sk = (const unsigned short*)A.ashift + ch0 * 4;
#pragma unroll
            for (int c = 0; c < 8; ++c) {
                float w[4];
#pragma unroll
                for (int j = 0; j < 4; ++j) w[j] = __uint_as_float((unsigned)sk[c * 4 + j] << 16);
                float kv[8];
#pragma unroll
                for (int t = 0; t < 8; ++t) {
                    float a = bfr(0.f + bfr(bfsel(kk[t], c) * w[3]));
                    a = bfr(a + bfr(bfsel(kk[t + 1], c) * w[2]));
                    a = bfr(a + bfr(bfsel(kk[t + 2], c) * w[1]));
                    a = bfr(a + bfr(bfsel(kk[t + 3], c) * w[0]));
                    kv[t] = a * bfsel(vv[t], c);
                }
                lw128(lds + A_U + c * A_UC + (tid & 3) * A_PS + (32 + (tid >> 2)) * 16,
                      v4u{pk2(kv[0], kv[1]), pk2(kv[2], kv[3]), pk2(kv[4], kv[5]), pk2(kv[6], kv[7])});
            }
        }
        v4u qq[8];
#pragma unroll
        for (int i = 0; i < 8; ++i) qq[i] = ld128(qr, (8 * tid + i) * 1024 + ch0 * 2);
        bar();

        for (int cc = 0; cc < 2; ++cc) {
            const int c = wave * 2 + cc;
            ldsp wb = lds + A_W + wave * A_WW;
            wait_lgkm0();
#pragma unroll
            for (int i = 0; i < 4; ++i) {
                const int m0 = 8 * (lane + 64 * i);
                const unsigned ro = (ch0 + c) * 4096 + m0 * 2;
                const v4u a = ld128(sr, ro);
                v2u pr = __builtin_amdgcn_raw_buffer_load_b64(sr, m0 == 0 ? ro : ro - 8, 0, 0);
                if (m0 == 0) pr = v2u{0, 0};
                const unsigned e0 = __builtin_amdgcn_alignbit(pr.y, pr.x, 16), e1 = __builtin_amdgcn_alignbit(a.x, pr.y, 16), e2 = __builtin_amdgcn_alignbit(a.y, a.x, 16),
                               e3 = __builtin_amdgcn_alignbit(a.z, a.y, 16), e4 = __builtin_amdgcn_alignbit(a.w, a.z, 16);
                ldsp d = wb + (32 + m0) * 2;
                lw128(d, a);
                lw128(d + A_CS, v4u{e1, e2, e3, e4});
                lw128(d + 2 * A_CS, v4u{pr.y, a.x, a.y, a.z});
                lw128(d + 3 * A_CS, v4u{e0, e1, e2, e3});
            }
            wait_lgkm0();
            v16f y0, y1;
#pragma unroll
            for (int j = 0; j < 16; ++j) { y0[j] = 0.f; y1[j] = 0.f; }
            ldsp ab = wb + q * A_CS + (32 + 16 * (G & 1) - 8 * kg + 4 * p - 20) * 2;
            ldsp ub = lds + A_U + c * A_UC + kg * A_PS + (1 + tb) * 16;
#pragma unroll 4
            for (int d = 0; d < 32; ++d) {
#pragma unroll
                for (int hf = 0; hf < 2; ++hf) {
                    ldsp a0 = ab + d * 64 + (20 - 16 * hf) * 2;
                    const v8bf A0 = as8(ltr(a0), ltr(a0 - 8)), A1 = as8(ltr(a0 + 2048), ltr(a0 + 2048 - 8));
                    ldsp b0 = ub + hf * 2 * A_PS + (31 - d) * 16;
                    const v8bf B0 = as8(lr128(b0)), B1 = as8(lr128(b0 + 512));
                    y0 = __builtin_amdgcn_mfma_f32_32x32x16_bf16(A0, B0, y0, 0, 0, 0);
                    y1 = __builtin_amdgcn_mfma_f32_32x32x16_bf16(A0, B1, y1, 0, 0, 0);
                    y1 = __builtin_amdgcn_mfma_f32_32x32x16_bf16(A1, B0, y1, 0, 0, 0);
                }
            }
            ldsp yb = lds + A_Y + c * A_YC + tb * 16 + kg * 8;
#pragma unroll
            for (int b = 0; b < 4; ++b) {
                lw64(yb + b * A_YS, v2u{pk2(y0[4 * b], y0[4 * b + 1]), pk2(y0[4 * b + 2], y0[4 * b + 3])});
                lw64(yb + b * A_YS + 512, v2u{pk2(y1[4 * b], y1[4 * b + 1]), pk2(y1[4 * b + 2], y1[4 * b + 3])});
            }
        }
        bar();

        {
            const unsigned short* dp = (const unsigned short*)A.ad + ch0;
            v4u o[8];
#pragma unroll
            for (int t = 0; t < 8; ++t) o[t] = v4u{0, 0, 0, 0};
#pragma unroll
            for (int c = 0; c < 8; ++c) {
                const float D = __uint_as_float((unsigned)dp[c] << 16);
                const v4u yv = lr128(lds + A_Y + c * A_YC + (tid & 3) * A_YS + (tid >> 2) * 16);
                const v4u kv = lr128(lds + A_U + c * A_UC + (tid & 3) * A_PS + (32 + (tid >> 2)) * 16);
#pragma unroll
                for (int t = 0; t < 8; ++t) {
                    const float y2 = bfr(bfsel(yv, t) + bfr(bfsel(kv, t) * D));
                    const unsigned r = pk2(bfsel(qq[t], c) * y2, 0.f) & 0xFFFFu;
                    o[t][c >> 1] |= (c & 1) ? (r << 16) : r;
                }
            }
#pragma unroll
            for (int t = 0; t < 8; ++t) *(v4u*)(A.out + (size_t)(8 * tid + t) * 2048 + ch0 * 2) = o[t];
        }
        bar();

    }
}

__global__ void __launch_bounds__(256, 1) fused_kernel(Args A) {
    extern __shared__ __attribute__((aligned(16))) unsigned char lds_raw[];
    ldsp lds = (ldsp)lds_raw;
    const int b = blockIdx.x, lane = threadIdx.x & 63;
    const int wave = __builtin_amdgcn_readfirstlane(threadIdx.x >> 6);
    const int slot = b >> 3, xc = b & 3;
    if ((b & 7) < 4) {
        const int idx = slot * 4 + xc;
        if (idx < 32) { h_role(A, lds, idx, wave, lane); }
        else if (idx < 96) {
            const rsrc_t ws = mkrsrc(A.ws, WS_END);
            if (idx == 40) {
                const unsigned beat = ld32c(ws, WS_HB);
                const v4u pv = {POISON, POISON, POISON, POISON};
#pragma unroll
                for (int i = 0; i < (int)(WS_INIT_END / 4096); ++i) st128c(ws, i * 4096 + threadIdx.x * 16, pv);
                wait_vm0();
                bar();
                if (wave == 3) beat_role(ws, __builtin_amdgcn_readfirstlane(beat), lane);
            } else {
                unsigned pr = 0;
                if (idx >= 48 && wave >= 2) {
                    const int w8 = (wave - 2) * 2;
                    pf2_part<1>(A, idx - 48, w8, wave, lane); pf2_part<1>(A, idx - 48, w8 + 1, wave, lane); pf2_part<8>(A, idx - 48, w8, wave, lane);
                    bar();
                    pf2_part<8>(A, idx - 48, w8 + 1, wave, lane);
                    pf2_part<2>(A, idx - 48, w8, wave, lane); pf2_part<2>(A, idx - 48, w8 + 1, wave, lane);
                    pf2_part<4>(A, idx - 48, w8, wave, lane); pf2_part<4>(A, idx - 48, w8 + 1, wave, lane);
                    wait_vm0();
                    return;
                }
                if (wave == 2 && idx < 40) pr = probe_role(A, idx - 32, lane);
                if (wave == 0) hb_wait(ws, lane);
                bar();
                if (wave == 2 && idx < 40 && lane == 0) st32c(ws, WS_PAR + (idx - 32) * 4, pr);
            }
            { if (wave < 2) r_role(A, (idx - 32) * 2 + wave, lane); }
        }
        else if (idx < 112) {
            pf2_part<1>(A, idx - 48, wave, wave, lane); pf2_part<8>(A, idx - 48, wave, wave, lane);
            pf2_part<2>(A, idx - 48, wave, wave, lane); pf2_part<4>(A, idx - 48, wave, wave, lane);
            wait_vm0();
            if (wave == 0) hb_wait(mkrsrc(A.ws, WS_END), lane); bar(); a_role(A, lds, idx - 96, wave, lane); }
        else { g_role<true>(A, lds, idx - 112 + 32, wave, lane); }
    } else {
        if (slot < 16) {
            pf2_part<3>(A, slot * 4 + xc, wave, wave, lane);
            if (wave == 0) hb_wait(mkrsrc(A.ws, WS_END), lane); bar(); wait_vm0(); o_role(A, lds, xc, slot, wave, lane); }
        else if (slot < 24) { g_role<false>(A, lds, (slot - 16) * 4 + xc, wave, lane); }
        else { if (wave == 0) hb_wait(mkrsrc(A.ws, WS_END), lane); bar(); x_role(A, (slot - 24) * 4 + xc, wave, lane); }
    }
}

extern "C" void kernel_launch(void* const* d_in, const int* in_sizes, int n_in, void* d_out, int out_size, void* d_ws, size_t ws_size,
                              hipStream_t stream) {
    Args a;
    a.aq = (const unsigned char*)d_in[0]; a.ak = (const unsigned char*)d_in[1]; a.av = (const unsigned char*)d_in[2];
    a.ashift = (const unsigned char*)d_in[3]; a.assm = (const unsigned char*)d_in[4]; a.ad = (const unsigned char*)d_in[5];
    a.bx = (const unsigned char*)d_in[6]; a.bmem = (const unsigned char*)d_in[7];
    a.wq = (const unsigned char*)d_in[8]; a.wk = (const unsigned char*)d_in[9]; a.wv = (const unsigned char*)d_in[10]; a.wo = (const unsigned char*)d_in[11];
    unsigned char* wsb = (unsigned char*)(((uintptr_t)d_ws + (2u << 20) - 1) & ~(uintptr_t)((2u << 20) - 1));
    a.out = (unsigned char*)d_out; a.ws = wsb;
    (void)hipFuncSetAttribute((const void*)fused_kernel, hipFuncAttributeMaxDynamicSharedMemorySize, LDS_BYTES);
    hipLaunchKernelGGL(fused_kernel, dim3(256), dim3(256), LDS_BYTES, stream, a);
}
```
